```python
import torch

def _softmax(x, axis=-1):
    return torch.softmax(x, dim=axis)
def _gelu(x):
    return torch.nn.functional.gelu(x, approximate="tanh")
def _silu(x):
    return torch.nn.functional.silu(x)
def _sigmoid(x):
    return torch.sigmoid(x)
def _layernorm(x, axis=-1, eps=1e-5):
    m = x.mean(dim=axis, keepdim=True)
    v = x.var(dim=axis, keepdim=True, unbiased=False)
    return (x - m) / torch.sqrt(v + eps)
def _scatter_add(tgt, idx, src, axis=0):
    return tgt.clone().index_add_(axis, idx, src)
def _segment_sum(src, seg, n):
    out = torch.zeros((n,) + src.shape[1:], dtype=src.dtype)
    return out.index_add_(0, seg, src)
def _pad(x, pw):
    flat = tuple(v for pair in reversed(pw) for v in pair)
    return torch.nn.functional.pad(x, flat)

kernel_name = 'gated_mix__linear_attn_block__dense_activation_gelu__prompt'

def setup_inputs():

    _w = (lambda a: (lambda t: t.to(torch.bfloat16) if t.is_floating_point() else t)(torch.as_tensor(a)))
    def _A_setup():
        B, S, D = 1, 2048, 640
        return {
            "x": _w((lambda *s: torch.randn(*s, dtype=torch.bfloat16) * 0.05)(B, S, D)),
            "wqkv": _w((lambda *s: torch.randn(*s, dtype=torch.bfloat16) * 0.8)(D, 3 * D)),
            "wo": _w((lambda *s: torch.randn(*s, dtype=torch.bfloat16) * 0.05)(D, D)),
        }
    _A_in = _A_setup()

    def _B_setup():
        B, S, D, F = 1, 2048, 640, 1728
        return {
            "x": _w((lambda *s: torch.randn(*s, dtype=torch.bfloat16) * 0.05)(B, S, D)),
            "w1": _w((lambda *s: torch.randn(*s, dtype=torch.bfloat16) * 3.2)(D, F) * (D ** -0.5)),
            "b1": _w((lambda *s: torch.randn(*s, dtype=torch.bfloat16) * 3.2)(F) * 0.01),
            "w2": _w((lambda *s: torch.randn(*s, dtype=torch.bfloat16) * 0.4)(F, D) * (F ** -0.5)),
            "b2": _w((lambda *s: torch.randn(*s, dtype=torch.bfloat16) * 0.8)(D) * 0.01),
        }
    _B_in = _B_setup()

    return {
        "A_x": _A_in["x"],
        "A_wqkv": _A_in["wqkv"],
        "A_wo": _A_in["wo"],
        "B_x": _B_in["x"],
        "B_w1": _B_in["w1"],
        "B_b1": _B_in["b1"],
        "B_w2": _B_in["w2"],
        "B_b2": _B_in["b2"],
        "gate_w": _w((lambda *s: torch.randn(*s, dtype=torch.bfloat16) * 0.05)(640) * 16.0),
    }

def reference(A_x, A_wqkv, A_wo, B_x, B_w1, B_b1, B_w2, B_b2, gate_w):
    def _A_ref(x, wqkv, wo):
        B, S, D = x.shape
        H = 32; Dh = D // H
        qkv = (lambda x, *d: x.permute(*d))((x @ wqkv).reshape(B, S, 3, H, Dh), 2, 0, 3, 1, 4)
        q, k, v = qkv[0], qkv[1], qkv[2]
        phi = lambda t: torch.where(t > 0, t + 1, torch.exp(t)) + 1e-6
        pq, pk = phi(q), phi(k)
        kv = torch.einsum("bhsd,bhse->bhde", pk, v)
        z = torch.einsum("bhsd->bhd", pk)
        num = torch.einsum("bhqd,bhde->bhqe", pq, kv)
        den = torch.einsum("bhqd,bhd->bhq", pq, z)[..., None] + 1e-6
        o = num / den
        return (lambda x, *d: x.permute(*d))(o, 0, 2, 1, 3).reshape(B, S, D) @ wo

    def _B_ref(x, w1, b1, w2, b2):
        h = torch.einsum("bsd,df->bsf", x, w1) + b1
        h = _gelu(h)
        return torch.einsum("bsf,fd->bsd", h, w2) + b2

    x = A_x
    g = _sigmoid((x * gate_w).sum(-1, keepdims=True))
    a = _A_ref(x, A_wqkv, A_wo)
    b = _B_ref(x, B_w1, B_b1, B_w2, B_b2)
    return g * a + (1 - g) * b
```

```cpp
#include <hip/hip_runtime.h>
#include <hip/hip_bf16.h>
#include <stdint.h>

namespace {

constexpr int S_ = 2048, D_ = 640, F_ = 1728;
typedef unsigned short u16;
typedef unsigned v4u __attribute__((ext_vector_type(4)));
typedef unsigned v2u __attribute__((ext_vector_type(2)));
typedef float f32x4 __attribute__((ext_vector_type(4)));
typedef float f32x2 __attribute__((ext_vector_type(2)));
typedef short bf16x8 __attribute__((ext_vector_type(8)));
typedef short bf16x4 __attribute__((ext_vector_type(4)));
typedef short v4i16_t __attribute__((ext_vector_type(4)));
typedef __bf16 bf2_t __attribute__((ext_vector_type(2)));
#define LAS __attribute__((address_space(3)))
#define GAS __attribute__((address_space(1)))
#define DI __device__ __forceinline__

constexpr size_t OFF_CNT = 0;
constexpr size_t OFF_G = 256;
constexpr size_t OFF_KVF = OFF_G + 2048 * 8;
constexpr size_t OFF_SLAB = OFF_KVF + 65536;
constexpr size_t SLAB_BYTES = (size_t)11 * 32 * 420 * 4;
constexpr size_t OFF_W2F = OFF_SLAB + SLAB_BYTES;
constexpr size_t OFF_WOF = OFF_W2F + (size_t)8 * 54 * 5 * 1024;
constexpr size_t OFF_PQ = OFF_WOF + (size_t)8 * 20 * 5 * 1024;
constexpr size_t OFF_HF = OFF_PQ + (size_t)128 * 8 * 2560;
constexpr size_t OFF_FB = OFF_HF + (size_t)128 * 54 * 1024;
constexpr size_t WS_BYTES = OFF_FB + (size_t)256 * 65536;
static_assert(OFF_SLAB % 256 == 0 && OFF_W2F % 256 == 0 && OFF_PQ % 256 == 0 && OFF_HF % 256 == 0, "align");
static_assert(WS_BYTES < (1ull << 31), "int offsets");

struct Params {
  const u16* x;
  const u16* wqkv;
  const u16* wo;
  const u16* w1;
  const u16* b1;
  const u16* w2;
  const u16* b2;
  const u16* gw;
  u16* out;
  unsigned char* ws;
};

DI float bf2f(unsigned v) { return __builtin_bit_cast(float, v << 16); }
DI float rbf(float a) { return (float)(__bf16)a; }
DI unsigned pack2(float a, float b) {
  f32x2 v = {a, b};
  bf2_t r = __builtin_convertvector(v, bf2_t);
  return __builtin_bit_cast(unsigned, r);
}
DI float phi_f(float acc) {
  const float t = rbf(acc);
  const float p = t > 0.f ? rbf(t + 1.f) : rbf(__expf(t));
  return rbf(p + 1e-6f);
}
DI float gelu_f(float x) {
  const float u = 0.7978845608028654f * (x + 0.044715f * x * x * x);
  return x * __builtin_amdgcn_rcpf(1.f + __expf(-2.f * u));
}
DI f32x4 mfma32(v4u a, v4u b, f32x4 c) {
  return __builtin_amdgcn_mfma_f32_16x16x32_bf16(__builtin_bit_cast(bf16x8, a), __builtin_bit_cast(bf16x8, b), c, 0, 0, 0);
}
DI f32x4 mfma16(v2u a, v2u b, f32x4 c) {
  return __builtin_amdgcn_mfma_f32_16x16x16bf16_1k(__builtin_bit_cast(bf16x4, a), __builtin_bit_cast(bf16x4, b), c, 0, 0, 0);
}
DI v2u tr_read(const unsigned char* p) {
  return __builtin_bit_cast(v2u, __builtin_amdgcn_ds_read_tr16_b64_v4i16((LAS v4i16_t*)p));
}
DI void glds16(const void* g, unsigned char* l) {
  __builtin_amdgcn_global_load_lds((const GAS unsigned*)g, (LAS unsigned*)l, 16, 0, 0);
}

struct TileMap { unsigned short v[256]; };
constexpr TileMap make_tilemap() {
  TileMap m{};
  for (int i = 0; i < 256; ++i) m.v[i] = 0xFFFF;
  for (int half = 0; half < 2; ++half) {
    unsigned short list[160] = {};
    int n = 0;
    const int q0 = half ? 6 : 0, q1 = half ? 11 : 6, h0 = half ? 6 : 0, h1 = half ? 13 : 6;
    for (int ct = 0; ct < 12; ++ct)
      for (int rt = q0; rt < q1; ++rt) list[n++] = (unsigned short)(((ct < 4 ? 0 : 1) << 12) | (rt << 6) | (ct < 4 ? ct : ct - 4));
    for (int ct = 0; ct < 9; ++ct)
      for (int rt = h0; rt < h1; ++rt) list[n++] = (unsigned short)((2 << 12) | (rt << 6) | ct);
    int pos = 0;
    for (int xq = 0; xq < 4; ++xq) {
      const int cnt = (n - pos + (3 - xq)) / (4 - xq);
      for (int i = 0; i < cnt; ++i) m.v[(half * 4 + xq) + 8 * i] = list[pos + i];
      pos += cnt;
    }
  }
  return m;
}
__device__ const TileMap g_tilemap = make_tilemap();

constexpr int K1_STAGE = 45056;
constexpr int K1_LDS = 3 * K1_STAGE;

template <int TYPE>
DI void k1_tile(const Params& p, unsigned char* smem, int rt, int ct) {
  constexpr int MT = TYPE == 2 ? 5 : 3, NTW = TYPE == 2 ? 3 : 5, WM = TYPE == 2 ? 2 : 4, WN = TYPE == 2 ? 4 : 2;
  constexpr int BM = MT * 16 * WM, BN = NTW * 16 * WN, NT = BN / 16, XB = BM * 128;
  static_assert(XB + 64 * BN * 2 == K1_STAGE, "stage");
  const int tid = threadIdx.x, lane = tid & 63, w = __builtin_amdgcn_readfirstlane(tid >> 6);
  const int wm = w / WN, wn = w % WN, g = lane >> 4, li = lane & 15;
  const int r0 = rt * BM;
  const u16* W = TYPE == 2 ? p.w1 : p.wqkv;
  constexpr int LDW = TYPE == 2 ? F_ : 3 * D_;
  const u16* xs[3];
  const u16* wsrc[3];
#pragma unroll
  for (int i = 0; i < 3; ++i) {
    int c = w * BM + 64 * i + lane;
    int row = c >> 3, slot = c & 7;
    int grow = r0 + row;
    grow = grow < S_ ? grow : S_ - 1;
    xs[i] = p.x + (size_t)grow * D_ + 8 * (slot ^ (row & 7));
    int cc = 64 * i + lane;
    int nt = cc >> 4, rowp = (cc >> 1) & 7, half = cc & 1;
    if (nt >= NT) nt = NT - 1;
    int k = 8 * w + (rowp ^ (4 * (w & 1)));
    int col = 16 * nt + 8 * half;
    int gcol;
    if (TYPE == 0) gcol = 160 * ct + col;
    else if (TYPE == 1) {
      int cwn = col / 80, c2 = col % 80;
      gcol = (c2 < 40 ? D_ + c2 : 2 * D_ + c2 - 40) + 80 * ct + 40 * cwn;
    } else gcol = 192 * ct + col;
    wsrc[i] = W + (size_t)k * LDW + gcol;
  }
  auto issue = [&](int kt, int buf) {
    unsigned char* sb = smem + buf * K1_STAGE;
    glds16(xs[0] + kt * 64, sb + (w * BM) * 16);
    glds16(xs[1] + kt * 64, sb + (w * BM + 64) * 16);
    if (BM == 192 || lane < 32) glds16(xs[2] + kt * 64, sb + (w * BM + 128) * 16);
    glds16(wsrc[0] + (size_t)kt * 64 * LDW, sb + XB + (w * 16 * NT) * 16);
    glds16(wsrc[1] + (size_t)kt * 64 * LDW, sb + XB + (w * 16 * NT + 64) * 16);
    if (NT == 12 || lane < 32) glds16(wsrc[2] + (size_t)kt * 64 * LDW, sb + XB + (w * 16 * NT + 128) * 16);
  };
  issue(0, 0);
  issue(1, 1);
  const unsigned xoff0 = (unsigned)((wm * MT * 16 + li) * 128 + ((g ^ (lane & 7)) << 4));
  const unsigned woff0 = (unsigned)(XB + g * NT * 256 + wn * NTW * 256 + (4 * (g & 1) + (li >> 2)) * 32 + (lane & 3) * 8);
  f32x4 acc[MT][NTW];
#pragma unroll
  for (int a = 0; a < MT; ++a)
#pragma unroll
    for (int b = 0; b < NTW; ++b) acc[a][b] = f32x4{0.f, 0.f, 0.f, 0.f};
  int buf = 0;
#pragma unroll 1
  for (int kt = 0; kt < 10; ++kt) {
    if (kt < 9) asm volatile("s_waitcnt vmcnt(6) lgkmcnt(0)" ::: "memory");
    else asm volatile("s_waitcnt vmcnt(0) lgkmcnt(0)" ::: "memory");
    __builtin_amdgcn_s_barrier();
    if (kt + 2 < 10) issue(kt + 2, buf == 0 ? 2 : buf - 1);
    const unsigned char* sb = smem + buf * K1_STAGE;
#pragma unroll
    for (int ks = 0; ks < 2; ++ks) {
      v4u xf[MT], wf[NTW];
#pragma unroll
      for (int a = 0; a < MT; ++a) xf[a] = *(const v4u*)(sb + (xoff0 ^ (ks * 64)) + a * 2048);
#pragma unroll
      for (int b = 0; b < NTW; ++b) {
        v2u lo = tr_read(sb + woff0 + ks * 4 * NT * 256 + b * 256);
        v2u hi = tr_read(sb + (woff0 ^ 128) + ks * 4 * NT * 256 + b * 256);
        wf[b] = v4u{lo.x, lo.y, hi.x, hi.y};
      }
#pragma unroll
      for (int a = 0; a < MT; ++a)
#pragma unroll
        for (int b = 0; b < NTW; ++b)
          acc[a][b] = TYPE == 1 ? mfma32(xf[a], wf[b], acc[a][b]) : mfma32(wf[b], xf[a], acc[a][b]);
    }
    buf = buf == 2 ? 0 : buf + 1;
  }
  if (TYPE == 0) {
    const int gq = 2 * ct + wn;
#pragma unroll
    for (int a = 0; a < MT; ++a) {
      const int st = (r0 + wm * 48 + 16 * a) >> 4;
      if (st < 128) {
        unsigned pk[5][2];
#pragma unroll
        for (int b = 0; b < 5; ++b) {
          pk[b][0] = pack2(phi_f(acc[a][b][0]), phi_f(acc[a][b][1]));
          pk[b][1] = pack2(phi_f(acc[a][b][2]), phi_f(acc[a][b][3]));
        }
        unsigned char* dst = p.ws + OFF_PQ + (size_t)(st * 8 + gq) * 2560;
        *(v4u*)(dst + lane * 16) = v4u{pk[0][0], pk[0][1], pk[1][0], pk[1][1]};
        *(v4u*)(dst + 1024 + lane * 16) = v4u{pk[2][0], pk[2][1], pk[3][0], pk[3][1]};
        *(v2u*)(dst + 2048 + lane * 8) = v2u{pk[4][0], pk[4][1]};
      }
    }
  } else if (TYPE == 2) {
    float bias[3][4];
#pragma unroll
    for (int b = 0; b < 3; ++b) {
      const v2u bb = *(const v2u*)(p.b1 + 192 * ct + 48 * wn + 16 * b + 4 * g);
      bias[b][0] = bf2f(bb.x & 0xFFFFu); bias[b][1] = bf2f(bb.x >> 16);
      bias[b][2] = bf2f(bb.y & 0xFFFFu); bias[b][3] = bf2f(bb.y >> 16);
    }
    const int ntg = 12 * ct + 3 * wn;
#pragma unroll
    for (int a = 0; a < MT; ++a) {
      const int st = (r0 + wm * 80 + 16 * a) >> 4;
      if (st < 128) {
        unsigned pk[3][2];
#pragma unroll
        for (int b = 0; b < 3; ++b) {
          float v[4];
#pragma unroll
          for (int r = 0; r < 4; ++r) v[r] = gelu_f(rbf(rbf(acc[a][b][r]) + bias[b][r]));
          pk[b][0] = pack2(v[0], v[1]);
          pk[b][1] = pack2(v[2], v[3]);
        }
        unsigned char* dst = p.ws + OFF_HF + (size_t)(st * 54 + (ntg >> 1)) * 1024 + lane * 16;
        if ((wn & 1) == 0) {
          *(v4u*)dst = v4u{pk[0][0], pk[0][1], pk[1][0], pk[1][1]};
          *(v2u*)(dst + 1024) = v2u{pk[2][0], pk[2][1]};
        } else {
          *(v2u*)(dst + 8) = v2u{pk[0][0], pk[0][1]};
          *(v4u*)(dst + 1024) = v4u{pk[1][0], pk[1][1], pk[2][0], pk[2][1]};
        }
      }
    }
  } else {
    v2u fr[3][5];
#pragma unroll
    for (int a = 0; a < 3; ++a) {
      const bool valid = (r0 + wm * 48 + 16 * a) < S_;
#pragma unroll
      for (int b = 0; b < 5; ++b) {
        const bool isk = (16 * b + li) < 40;
        float v[4];
#pragma unroll
        for (int r = 0; r < 4; ++r) {
          const float t = isk ? phi_f(acc[a][b][r]) : acc[a][b][r];
          v[r] = valid ? t : 0.f;
        }
        fr[a][b] = v2u{pack2(v[0], v[1]), pack2(v[2], v[3])};
      }
    }
    const v2u ones = v2u{0x3F803F80u, 0x3F803F80u};
    f32x4 d[10];
#pragma unroll
    for (int i = 0; i < 10; ++i) d[i] = f32x4{0.f, 0.f, 0.f, 0.f};
#pragma unroll
    for (int a = 0; a < 3; ++a) {
      d[0] = mfma16(fr[a][0], fr[a][2], d[0]);
      d[1] = mfma16(fr[a][0], fr[a][3], d[1]);
      d[2] = mfma16(fr[a][1], fr[a][2], d[2]);
      d[3] = mfma16(fr[a][1], fr[a][3], d[3]);
      d[4] = mfma16(fr[a][1], fr[a][4], d[4]);
      d[5] = mfma16(fr[a][2], fr[a][3], d[5]);
      d[6] = mfma16(fr[a][2], fr[a][4], d[6]);
      d[7] = mfma16(fr[a][0], ones, d[7]);
      d[8] = mfma16(fr[a][1], ones, d[8]);
      d[9] = mfma16(fr[a][2], ones, d[9]);
    }
    __syncthreads();
    f32x4* red = (f32x4*)smem;
#pragma unroll
    for (int i = 0; i < 10; ++i) red[(w * 10 + i) * 64 + lane] = d[i];
    __syncthreads();
    const float* redf = (const float*)smem;
    float* slab = (float*)(p.ws + OFF_SLAB) + (size_t)(rt * 32 + 4 * ct) * 420;
    for (int idx = tid; idx < 1680; idx += 512) {
      const int hl = idx / 420, rem = idx % 420, dd = rem / 21, e = rem % 21;
      const int cwn = hl >> 1, hh = hl & 1;
      const int c1 = 20 * hh + dd, n1 = c1 >> 4, row = c1 & 15;
      int tile, col;
      if (e < 20) {
        const int c2 = 40 + 20 * hh + e, n2 = c2 >> 4;
        col = c2 & 15;
        tile = n1 == 0 ? n2 - 2 : (n1 == 1 ? n2 : n2 + 2);
      } else {
        tile = 7 + n1;
        col = 0;
      }
      const int off = tile * 256 + ((row >> 2) * 16 + col) * 4 + (row & 3);
      float s = 0.f;
#pragma unroll
      for (int m = 0; m < 4; ++m) s += redf[(m * 2 + cwn) * 2560 + off];
      slab[idx] = s;
    }
  }
}

DI void k1_side(const Params& p) {
  const int tid = threadIdx.x, lane = tid & 63, w = __builtin_amdgcn_readfirstlane(tid >> 6);
  const int gw = blockIdx.x * 8 + w, g = lane >> 4, li = lane & 15;
  {
    const unsigned* xr = (const unsigned*)(p.x + (size_t)gw * D_) + lane * 5;
    const unsigned* gr = (const unsigned*)p.gw + lane * 5;
    float s = 0.f;
#pragma unroll
    for (int i = 0; i < 5; ++i) {
      const unsigned a = xr[i], b = gr[i];
      s += rbf(bf2f(a & 0xFFFFu) * bf2f(b & 0xFFFFu));
      s += rbf(bf2f(a >> 16) * bf2f(b >> 16));
    }
#pragma unroll
    for (int o = 32; o > 0; o >>= 1) s += __shfl_xor(s, o);
    if (lane == 0) {
      const float lg = rbf(s);
      const float gg = rbf(1.f / (1.f + __expf(-lg)));
      *(f32x2*)(p.ws + OFF_G + (size_t)gw * 8) = f32x2{gg, rbf(1.f - gg)};
    }
  }
  for (int id = gw; id < 2960; id += 2048) {
    const int T = id / 40, ntg = id % 40, nb = ntg / 5, t = ntg % 5;
    const int col = 16 * ntg + li;
    unsigned v[8];
    unsigned char* dst;
    if (T < 54) {
#pragma unroll
      for (int j = 0; j < 8; ++j) v[j] = p.w2[(size_t)(32 * T + 16 * (j >> 2) + 4 * g + (j & 3)) * D_ + col];
      dst = p.ws + OFF_W2F + (size_t)((nb * 54 + T) * 5 + t) * 1024;
    } else {
      const int U = T - 54, ww = U / 5, u = U % 5;
#pragma unroll
      for (int j = 0; j < 8; ++j) {
        const int vv = 8 * u + j, hl = vv / 5, i = vv % 5;
        v[j] = p.wo[(size_t)((8 * ww + hl) * 20 + 5 * g + i) * D_ + col];
      }
      dst = p.ws + OFF_WOF + (size_t)((nb * 20 + U) * 5 + t) * 1024;
    }
    *(v4u*)(dst + lane * 16) = v4u{v[0] | (v[1] << 16), v[2] | (v[3] << 16), v[4] | (v[5] << 16), v[6] | (v[7] << 16)};
  }
  if (blockIdx.x == 0 && tid == 0) *(unsigned*)(p.ws + OFF_CNT) = 0u;
}

__global__ void __launch_bounds__(512, 2) k1_kernel(Params p) {
  extern __shared__ __attribute__((aligned(16))) unsigned char smem[];
  k1_side(p);
  const unsigned t = g_tilemap.v[blockIdx.x];
  if (t == 0xFFFFu) return;
  const int type = t >> 12, rt = (t >> 6) & 63, ct = t & 63;
  if (type == 0) k1_tile<0>(p, smem, rt, ct);
  else if (type == 1) k1_tile<1>(p, smem, rt, ct);
  else k1_tile<2>(p, smem, rt, ct);
}

constexpr int AUX_SC1 = 16;
constexpr int RSRC_W3 = 0x00020000;
constexpr int K2_LDS = 4 * 20 * 1024;

DI unsigned kvf_pair(const float* slab, int head, int ls, int jp) {
  const int f = ls >> 6, l2 = ls & 63, rho = l2 & 15, g2 = l2 >> 4, hh = head & 3;
  int e;
  if (f == 0) e = 5 * (rho >> 2) + (rho & 3);
  else e = (rho & 3) == 0 ? 5 * (rho >> 2) + 4 : ((rho & 3) == 1 ? 20 : -1);
  float v[2];
#pragma unroll
  for (int q = 0; q < 2; ++q) {
    const int j = 2 * jp + q;
    const int d = 16 * (j >> 2) + 4 * g2 + (j & 3) - 4 * hh;
    const bool ok = (e >= 0) && (d >= 0) && (d < 20);
    const int off = ok ? d * 21 + e : 0;
    float s = 0.f;
#pragma unroll
    for (int pp = 0; pp < 11; ++pp) s += slab[(size_t)(pp * 32 + head) * 420 + off];
    v[q] = ok ? s : 0.f;
  }
  return pack2(v[0], v[1]);
}

__global__ void __launch_bounds__(256, 1) k2_kernel(Params p) {
  extern __shared__ __attribute__((aligned(16))) unsigned char smem[];
  const int tid = threadIdx.x, lane = tid & 63, w = __builtin_amdgcn_readfirstlane(tid >> 6);
  const int g = lane >> 4, li = lane & 15;
  const int b = blockIdx.x, bi = b >> 3;
  const int rb = 4 * (b & 7) + (bi >> 3), nb = bi & 7;
  const __amdgpu_buffer_rsrc_t rs = __builtin_amdgcn_make_buffer_rsrc((void*)p.ws, 0, (int)WS_BYTES, RSRC_W3);
  const float* slab = (const float*)(p.ws + OFF_SLAB);
  GAS unsigned* cnt = (GAS unsigned*)(p.ws + OFF_CNT);
  if (w == 0) {
    const int head = b >> 3, ls = (b & 7) * 16 + (lane >> 2), jp = lane & 3;
    const unsigned v = kvf_pair(slab, head, ls, jp);
    __builtin_amdgcn_raw_buffer_store_b32(v, rs, (int)OFF_KVF + head * 2048 + ls * 16 + jp * 4, 0, AUX_SC1);
    asm volatile("s_waitcnt vmcnt(0)" ::: "memory");
    if (lane == 0) __hip_atomic_fetch_add(cnt, 1u, __ATOMIC_RELAXED, __HIP_MEMORY_SCOPE_AGENT);
  }
  constexpr int NSTEP = 14, DEPTH = 6;
  const int T0 = w < 2 ? 14 * w : 28 + 13 * (w - 2);
  const int cntw = w < 2 ? 14 : 13;
  const int vo = lane * 16;
  const int hbase = (int)OFF_HF + (4 * rb * 54) * 1024;
  const int wbase = (int)OFF_W2F + (nb * 54 * 5) * 1024;
  v4u hf[DEPTH][4], wf[DEPTH][5];
  auto ld_step = [&](int i, v4u (&h)[4], v4u (&ww)[5]) {
    int T = T0 + i;
    T = T < 54 ? T : 53;
#pragma unroll
    for (int m = 0; m < 4; ++m) h[m] = __builtin_amdgcn_raw_buffer_load_b128(rs, vo, hbase + (m * 54 + T) * 1024, 0);
#pragma unroll
    for (int t = 0; t < 5; ++t) ww[t] = __builtin_amdgcn_raw_buffer_load_b128(rs, vo, wbase + (T * 5 + t) * 1024, 0);
  };
#pragma unroll
  for (int i = 0; i < DEPTH; ++i) ld_step(i, hf[i], wf[i]);
  __builtin_amdgcn_sched_barrier(0);
  f32x2 gv[4];
#pragma unroll
  for (int m = 0; m < 4; ++m) gv[m] = *(const f32x2*)(p.ws + OFF_G + (size_t)(64 * rb + 16 * m + li) * 8);
  f32x4 acc[5][4];
#pragma unroll
  for (int t = 0; t < 5; ++t)
#pragma unroll
    for (int m = 0; m < 4; ++m) acc[t][m] = f32x4{0.f, 0.f, 0.f, 0.f};
#pragma unroll
  for (int i = 0; i < NSTEP; ++i) {
    if (i < 13 || cntw == 14) {
#pragma unroll
      for (int t = 0; t < 5; ++t)
#pragma unroll
        for (int m = 0; m < 4; ++m) acc[t][m] = mfma32(wf[i % DEPTH][t], hf[i % DEPTH][m], acc[t][m]);
    }
    if (i + DEPTH < NSTEP) ld_step(i + DEPTH, hf[i % DEPTH], wf[i % DEPTH]);
    __builtin_amdgcn_sched_barrier(0);
  }
#pragma unroll
  for (int t = 0; t < 5; ++t)
#pragma unroll
    for (int m = 0; m < 4; ++m) acc[t][m] *= gv[m].y;
  v4u pa[4][2], pb[4][2];
  v2u pc[4][2];
#pragma unroll
  for (int m = 0; m < 4; ++m)
#pragma unroll
    for (int gr = 0; gr < 2; ++gr) {
      const int o = (int)OFF_PQ + ((4 * rb + m) * 8 + 2 * w + gr) * 2560;
      pa[m][gr] = __builtin_amdgcn_raw_buffer_load_b128(rs, vo, o, 0);
      pb[m][gr] = __builtin_amdgcn_raw_buffer_load_b128(rs, vo, o + 1024, 0);
      pc[m][gr] = __builtin_amdgcn_raw_buffer_load_b64(rs, lane * 8, o + 2048, 0);
    }
  v4u wo[5][5];
#pragma unroll
  for (int u = 0; u < 5; ++u)
#pragma unroll
    for (int t = 0; t < 5; ++t)
      wo[u][t] = __builtin_amdgcn_raw_buffer_load_b128(rs, vo, (int)OFF_WOF + ((nb * 20 + 5 * w + u) * 5 + t) * 1024, 0);
  int kvbase = (int)OFF_KVF + 8 * w * 2048;
  {
    bool ok = false;
    for (int spins = 0; spins < 200000; ++spins) {
      const unsigned c = __hip_atomic_load(cnt, __ATOMIC_RELAXED, __HIP_MEMORY_SCOPE_AGENT);
      if (__builtin_amdgcn_readfirstlane(c) >= 256u) { ok = true; break; }
      __builtin_amdgcn_s_sleep(2);
    }
    __builtin_amdgcn_fence(__ATOMIC_ACQUIRE, "wavefront");
    if (!ok) {
      const int fb = (int)OFF_FB + b * 65536 + 8 * w * 2048;
      for (int it = 0; it < 8 * 8; ++it) {
        const int hl = it >> 3, ls = (it & 7) * 16 + (lane >> 2), jp = lane & 3;
        const unsigned v = kvf_pair(slab, 8 * w + hl, ls, jp);
        __builtin_amdgcn_raw_buffer_store_b32(v, rs, fb + hl * 2048 + ls * 16 + jp * 4, 0, AUX_SC1);
      }
      asm volatile("s_waitcnt vmcnt(0)" ::: "memory");
      kvbase = fb;
    }
  }
  v4u kvf[8][2];
#pragma unroll
  for (int hl = 0; hl < 8; ++hl)
#pragma unroll
    for (int f = 0; f < 2; ++f) kvf[hl][f] = __builtin_amdgcn_raw_buffer_load_b128(rs, vo, kvbase + hl * 2048 + f * 1024, AUX_SC1);
#pragma unroll
  for (int m = 0; m < 4; ++m) {
    float ov[40];
#pragma unroll
    for (int hl = 0; hl < 8; ++hl) {
      const int gr = hl >> 2, hh = hl & 3;
      v4u bf;
      if (hh == 0) bf = pa[m][gr];
      else if (hh == 1) bf = v4u{pa[m][gr].z, pa[m][gr].w, pb[m][gr].x, pb[m][gr].y};
      else if (hh == 2) bf = pb[m][gr];
      else bf = v4u{pb[m][gr].z, pb[m][gr].w, pc[m][gr].x, pc[m][gr].y};
      const f32x4 z4 = f32x4{0.f, 0.f, 0.f, 0.f};
      const f32x4 c0 = mfma32(kvf[hl][0], bf, z4);
      const f32x4 c1 = mfma32(kvf[hl][1], bf, z4);
      const float sc = gv[m].x / (c1[1] + 1e-6f);
      ov[5 * hl + 0] = c0[0] * sc;
      ov[5 * hl + 1] = c0[1] * sc;
      ov[5 * hl + 2] = c0[2] * sc;
      ov[5 * hl + 3] = c0[3] * sc;
      ov[5 * hl + 4] = c1[0] * sc;
    }
#pragma unroll
    for (int u = 0; u < 5; ++u) {
      const v4u of = v4u{pack2(ov[8 * u], ov[8 * u + 1]), pack2(ov[8 * u + 2], ov[8 * u + 3]),
                         pack2(ov[8 * u + 4], ov[8 * u + 5]), pack2(ov[8 * u + 6], ov[8 * u + 7])};
#pragma unroll
      for (int t = 0; t < 5; ++t) acc[t][m] = mfma32(wo[u][t], of, acc[t][m]);
    }
  }
  f32x4* red = (f32x4*)smem;
#pragma unroll
  for (int t = 0; t < 5; ++t)
#pragma unroll
    for (int m = 0; m < 4; ++m) red[((w * 4 + m) * 5 + t) * 64 + lane] = acc[t][m];
  __syncthreads();
  const float omg = w == 0 ? gv[0].y : (w == 1 ? gv[1].y : (w == 2 ? gv[2].y : gv[3].y));
  u16* orow = p.out + (size_t)(64 * rb + 16 * w + li) * D_ + 80 * nb + 4 * g;
#pragma unroll
  for (int t = 0; t < 5; ++t) {
    f32x4 s = red[((0 * 4 + w) * 5 + t) * 64 + lane];
#pragma unroll
    for (int ww = 1; ww < 4; ++ww) s += red[((ww * 4 + w) * 5 + t) * 64 + lane];
    const v2u bb = *(const v2u*)(p.b2 + 80 * nb + 16 * t + 4 * g);
    const float o0 = s[0] + omg * bf2f(bb.x & 0xFFFFu), o1 = s[1] + omg * bf2f(bb.x >> 16);
    const float o2 = s[2] + omg * bf2f(bb.y & 0xFFFFu), o3 = s[3] + omg * bf2f(bb.y >> 16);
    *(v2u*)(orow + 16 * t) = v2u{pack2(o0, o1), pack2(o2, o3)};
  }
}

}

extern "C" void kernel_launch(void* const* d_in, const int* in_sizes, int n_in, void* d_out, int out_size, void* d_ws,
                              size_t ws_size, hipStream_t stream) {
  Params p;
  p.x = (const u16*)d_in[0];
  p.wqkv = (const u16*)d_in[1];
  p.wo = (const u16*)d_in[2];
  p.w1 = (const u16*)d_in[4];
  p.b1 = (const u16*)d_in[5];
  p.w2 = (const u16*)d_in[6];
  p.b2 = (const u16*)d_in[7];
  p.gw = (const u16*)d_in[8];
  p.out = (u16*)d_out;
  p.ws = (unsigned char*)d_ws;
  (void)hipFuncSetAttribute((const void*)k1_kernel, hipFuncAttributeMaxDynamicSharedMemorySize, K1_LDS);
  (void)hipFuncSetAttribute((const void*)k2_kernel, hipFuncAttributeMaxDynamicSharedMemorySize, K2_LDS);
  k1_kernel<<<256, 512, K1_LDS, stream>>>(p);
  k2_kernel<<<256, 256, K2_LDS, stream>>>(p);
}
```

```cpp
#include <hip/hip_runtime.h>
#include <hip/hip_bf16.h>
#include <stdint.h>
#include <type_traits>
#include <utility>

namespace {

constexpr int S_ = 2048, D_ = 640, F_ = 1728;
typedef unsigned short u16;
typedef unsigned v4u __attribute__((ext_vector_type(4)));
typedef unsigned v2u __attribute__((ext_vector_type(2)));
typedef float f32x4 __attribute__((ext_vector_type(4)));
typedef float f32x2 __attribute__((ext_vector_type(2)));
typedef short bf16x8 __attribute__((ext_vector_type(8)));
typedef short bf16x4 __attribute__((ext_vector_type(4)));
typedef short v4i16_t __attribute__((ext_vector_type(4)));
typedef __bf16 bf2_t __attribute__((ext_vector_type(2)));
#define LAS __attribute__((address_space(3)))
#define GAS __attribute__((address_space(1)))
#define DI __device__ __forceinline__

constexpr size_t OFF_CNT = 0;
constexpr size_t OFF_G = 32 * 256;
constexpr size_t OFF_KVF = OFF_G + 2048 * 8;
constexpr size_t OFF_SLAB = OFF_KVF + 65536;
constexpr size_t SLAB_BYTES = ((size_t)11 * 32 * 420 * 4 + 255) / 256 * 256;
constexpr size_t OFF_W2F = OFF_SLAB + SLAB_BYTES;
constexpr size_t OFF_WOF = OFF_W2F + (size_t)8 * 54 * 5 * 1024;
constexpr size_t OFF_PQ = OFF_WOF + (size_t)8 * 20 * 5 * 1024;
constexpr size_t OFF_HF = OFF_PQ + (size_t)128 * 8 * 2560;
constexpr size_t OFF_FB = OFF_HF + (size_t)128 * 54 * 1024;
constexpr size_t OFF_DUMP = OFF_FB + (size_t)256 * 65536;
constexpr size_t WS_BYTES = OFF_DUMP + 65536;
static_assert(OFF_SLAB % 256 == 0 && OFF_W2F % 256 == 0 && OFF_PQ % 256 == 0 && OFF_HF % 256 == 0, "align");
static_assert(WS_BYTES < (1ull << 31), "int offsets");

struct Params {
  const u16* x;
  const u16* wqkv;
  const u16* wo;
  const u16* w1;
  const u16* b1;
  const u16* w2;
  const u16* b2;
  const u16* gw;
  u16* out;
  unsigned char* ws;
};

template <int... I, class Fn> DI void static_for_impl(std::integer_sequence<int, I...>, Fn&& fn) { (fn(std::integral_constant<int, I>{}), ...); }
template <int N, class Fn> DI void static_for(Fn&& fn) { static_for_impl(std::make_integer_sequence<int, N>{}, fn); }
DI float bf2f(unsigned v) { return __builtin_bit_cast(float, v << 16); }
DI float rbf(float a) { return (float)(__bf16)a; }
DI unsigned pack2(float a, float b) {
  f32x2 v = {a, b};
  bf2_t r = __builtin_convertvector(v, bf2_t);
  return __builtin_bit_cast(unsigned, r);
}
DI float phi_f(float t) {
  const float e = __builtin_amdgcn_exp2f(t * 1.4426950408889634f);
  return (t > 0.f ? t + 1.f : e) + 1e-6f;
}
DI f32x2 gelu2(f32x2 x) {
  const f32x2 c1 = {-2.302208198f, -2.302208198f}, c2 = {-0.1029432397f, -0.1029432397f};
  const f32x2 e = x * (x * x * c2 + c1);
  const f32x2 d = f32x2{__builtin_amdgcn_exp2f(e.x), __builtin_amdgcn_exp2f(e.y)} + f32x2{1.f, 1.f};
  return x * f32x2{__builtin_amdgcn_rcpf(d.x), __builtin_amdgcn_rcpf(d.y)};
}
DI f32x4 mfma32(v4u a, v4u b, f32x4 c) {
  return __builtin_amdgcn_mfma_f32_16x16x32_bf16(__builtin_bit_cast(bf16x8, a), __builtin_bit_cast(bf16x8, b), c, 0, 0, 0);
}
DI f32x4 mfma16(v2u a, v2u b, f32x4 c) {
  return __builtin_amdgcn_mfma_f32_16x16x16bf16_1k(__builtin_bit_cast(bf16x4, a), __builtin_bit_cast(bf16x4, b), c, 0, 0, 0);
}
DI v2u tr_read(const unsigned char* p) {
  return __builtin_bit_cast(v2u, __builtin_amdgcn_ds_read_tr16_b64_v4i16((LAS v4i16_t*)p));
}
DI void glds16(unsigned voff, const void* sbase, unsigned lds_dst) {
  unsigned keep;
  asm volatile("s_mov_b32 %0, m0\n\ts_mov_b32 m0, %3\n\ts_nop 0\n\tglobal_load_lds_dwordx4 %1, %2\n\ts_mov_b32 m0, %0"
               : "=&s"(keep) : "v"(voff), "s"(sbase), "s"(lds_dst) : "memory");
}
#define STAMP(i)
#define STAMP_DECL
#define STAMP_PARAM
#define STAMP_ARG
#define STAMP_STORE(base, nw)
constexpr size_t OFF_DBG1 = (size_t)64 << 20, OFF_DBG2 = (size_t)65 << 20;

struct TileMap { unsigned v[256]; };
constexpr TileMap make_tilemap() {
  TileMap m{};
  for (int i = 0; i < 256; ++i) m.v[i] = 0xFFFF;
  for (int half = 0; half < 2; ++half) {
    unsigned short list[160] = {};
    int n = 0;
    const int q0 = half ? 6 : 0, q1 = half ? 11 : 6, h0 = half ? 6 : 0, h1 = half ? 13 : 6;
    for (int ct = 0; ct < 12; ++ct)
      for (int rt = q0; rt < q1; ++rt) list[n++] = (unsigned short)(((ct < 4 ? 0 : 1) << 12) | (rt << 6) | (ct < 4 ? ct : ct - 4));
    for (int ct = 0; ct < 9; ++ct)
      for (int rt = h0; rt < h1; ++rt) list[n++] = (unsigned short)((2 << 12) | (rt << 6) | ct);
    int pos = 0;
    for (int xq = 0; xq < 4; ++xq) {
      const int cnt = (n - pos + (3 - xq)) / (4 - xq);
      for (int i = 0; i < cnt; ++i) m.v[(half * 4 + xq) + 8 * i] = list[pos + i];
      pos += cnt;
    }
  }
  for (int b = 0; b < 256; ++b) {
    const unsigned t = m.v[b] & 0xFFFF;
    if (t == 0xFFFF) continue;
    int rr = 0, rc = 0, cr = 0, cc = 0;
    for (int o = b & 7; o < 256; o += 8) {
      const unsigned u = m.v[o] & 0xFFFF;
      if (u == 0xFFFF) continue;
      if (((u >> 12) == 2) == ((t >> 12) == 2) && ((u >> 6) & 63) == ((t >> 6) & 63)) { if (o < b) ++rr; ++rc; }
      if ((u >> 12) == (t >> 12) && (u & 63) == (t & 63)) { if (o < b) ++cr; ++cc; }
    }
    m.v[b] = t | (unsigned)(rr | rc << 4) << 16 | (unsigned)(cr | cc << 4) << 24;
  }
  return m;
}
struct Params1 {
  Params p;
  TileMap tm;
};

constexpr int K1_STAGE = 45056;
constexpr int K1_LDS = 3 * K1_STAGE + 512 + 16384;

template <bool BARS> DI void k1_side(const Params& p, unsigned char* smem, const u16* bias);

template <int TYPE>
DI void k1_tile(const Params& p, unsigned char* smem, int rt, int ct, unsigned share STAMP_PARAM) {
  constexpr int MT = TYPE == 2 ? 5 : 3, NTW = TYPE == 2 ? 3 : 5, WM = TYPE == 2 ? 2 : 4, WN = TYPE == 2 ? 4 : 2;
  constexpr int BM = MT * 16 * WM, BN = NTW * 16 * WN, NT = BN / 16, XB = BM * 128, NMM = MT * NTW;
  constexpr int NX = BM / 32, NW = NT / 2;
  constexpr int NRD = MT + 2 * NTW;
  static_assert(XB + 64 * BN * 2 == K1_STAGE && NX + NW == 11, "stage");
  const int tid = threadIdx.x, lane = tid & 63, w = __builtin_amdgcn_readfirstlane(tid >> 6);
  const int wm = w / WN, wn = w % WN, g = lane >> 4, li = lane & 15;
  if (w == 11) {
    k1_side<true>(p, smem, TYPE == 2 ? p.b1 + 192 * ct : nullptr);
    return;
  }
  if (w == 10) {
    const int rv_ = rt * BM, r0_ = rv_ + BM <= S_ ? rv_ : S_ - BM;
    const int rr = share & 15, rc = (share >> 4) & 15, cr = (share >> 8) & 15, cc = (share >> 12) & 15;
    unsigned dummy = 0;
    const unsigned char* Wb = (const unsigned char*)(TYPE == 2 ? p.w1 : p.wqkv);
    constexpr int LDWB = 2 * (TYPE == 2 ? F_ : 3 * D_);
    const int c = lane & 3;
    int o;
    if (TYPE == 0) o = 320 * ct + (c < 3 ? 128 * c : 316);
    else if (TYPE == 1) o = (c < 2 ? 2 * D_ : 4 * D_) + 160 * ct + (c & 1) * 156;
    else o = 384 * ct + (c < 3 ? 128 * c : 256);
#pragma unroll 1
    for (int bi = 0; bi < 4; ++bi) {
      __builtin_amdgcn_s_barrier();
#pragma unroll 1
      for (int kt = 3 + 2 * bi; kt < 5 + 2 * bi && kt < 10; ++kt) {
        for (int r = rr + rc * lane; r < BM; r += rc * 64) {
          const unsigned char* a = (const unsigned char*)p.x + (size_t)(r0_ + r) * (2 * D_) + kt * 128;
          asm volatile("global_load_dword %0, %1, off" : "+v"(dummy) : "v"(a) : "memory");
        }
        for (int r = cr + cc * (lane >> 2); r < 64; r += cc * 16) {
          const unsigned char* a = Wb + (size_t)(64 * kt + r) * LDWB + o;
          asm volatile("global_load_dword %0, %1, off" : "+v"(dummy) : "v"(a) : "memory");
        }
      }
    }
    __builtin_amdgcn_s_barrier();
    __builtin_amdgcn_s_barrier();
    asm volatile("s_waitcnt vmcnt(0)" : "+v"(dummy) :: "memory");
    return;
  }
  const int rv = rt * BM, r0 = rv + BM <= S_ ? rv : S_ - BM;
  const u16* W = TYPE == 2 ? p.w1 : p.wqkv;
  constexpr int LDW = TYPE == 2 ? F_ : 3 * D_;
  const unsigned srcx = (unsigned)((r0 + (lane >> 3)) * D_ + 8 * ((lane & 7) ^ (lane >> 3))) * 2u;
  unsigned src[NW];
#pragma unroll
  for (int i = 0; i < NW; ++i) {
    const int cc = 64 * i + lane;
    const int kbl = cc / (16 * NT), c2 = cc % (16 * NT);
    const int nt = c2 >> 4, rowp = (c2 >> 1) & 7, half = c2 & 1;
    const int k = 8 * kbl + (rowp ^ (4 * kbl));
    const int col = 16 * nt + 8 * half;
    int gcol;
    if (TYPE == 0) gcol = 160 * ct + col;
    else if (TYPE == 1) {
      gcol = (col < 80 ? D_ + col : 2 * D_ + col - 80) + 80 * ct;
    } else gcol = 192 * ct + col;
    src[i] = (unsigned)(k * LDW + gcol) * 2u;
  }
  const unsigned lds0 = (unsigned)(uintptr_t)(LAS unsigned char*)smem;
  auto issue1 = [&](int i, int wv, int kt, unsigned sb) {
    if (i < NX) glds16(srcx, p.x + kt * 64 + (wv * (BM / 4) + i * 8) * D_, sb + (wv * BM * 2 + 64 * i) * 16);
    else glds16(src[i - NX], W + (size_t)(kt * 64 + wv * 16) * LDW, sb + XB + (wv * 32 * NT + 64 * (i - NX)) * 16);
  };
  if (w >= 8) {
    const int q0 = 2 * (w - 8);
#pragma unroll
    for (int t = 1; t < 3; ++t)
#pragma unroll
      for (int i = 0; i < 22; ++i) issue1(i % 11, q0 + i / 11, t, lds0 + t * K1_STAGE);
    __builtin_amdgcn_s_barrier();
    int bf = 0;
#pragma unroll 1
    for (int kt = 0; kt < 9; ++kt) {
      if (kt < 8) asm volatile("s_waitcnt vmcnt(22)" ::: "memory");
      else asm volatile("s_waitcnt vmcnt(0)" ::: "memory");
      __builtin_amdgcn_s_barrier();
      if (kt + 3 < 10) {
#pragma unroll
        for (int i = 0; i < 22; ++i) issue1(i % 11, q0 + i / 11, kt + 3, lds0 + bf * K1_STAGE);
      }
      bf = bf == 2 ? 0 : bf + 1;
    }
    return;
  }
#pragma unroll
  for (int i = 0; i < 11; ++i)
    if ((i & 1) == (w & 1)) issue1(i, w >> 1, 0, lds0);
  STAMP(1);
  const unsigned xoff0 = (unsigned)((wm * MT * 16 + li) * 128 + ((g ^ (lane & 7)) << 4));
  const unsigned woff0 = (unsigned)(XB + g * NT * 256 + wn * NTW * 256 + (4 * (g & 1) + (li >> 2)) * 32 + (lane & 3) * 8);
  unsigned woffk[5];
#pragma unroll
  for (int b = 0; b < 5; ++b) {
    const int cc = 16 * b + 4 * (lane & 3);
    const int col = cc < 40 ? 40 * wn + cc : 40 + 40 * wn + cc;
    woffk[b] = (unsigned)(XB + g * NT * 256 + (col >> 4) * 256 + (4 * (g & 1) + (li >> 2)) * 32 + (col & 15) * 2);
  }
  unsigned char* wsb = p.ws;
  asm volatile("" : "+s"(wsb));
  f32x4 acc[MT][NTW];
#pragma unroll
  for (int a = 0; a < MT; ++a)
#pragma unroll
    for (int b = 0; b < NTW; ++b) acc[a][b] = f32x4{0.f, 0.f, 0.f, 0.f};
  v4u xa[MT], xb[MT];
  v2u wa[NTW][2], wb[NTW][2];
  auto rd1 = [&](int r, int ks, const unsigned char* sb, v4u (&xf)[MT], v2u (&wf)[NTW][2]) {
    if (r < MT) xf[r] = *(const v4u*)(sb + (xoff0 ^ (ks * 64)) + r * 2048);
    else {
      const int b = (r - MT) >> 1, hi = (r - MT) & 1;
      if (TYPE == 1) wf[b][hi] = tr_read(sb + (woffk[b < 5 ? b : 0] ^ (hi * 128)) + ks * 4 * NT * 256);
      else wf[b][hi] = tr_read(sb + (woff0 ^ (hi * 128)) + ks * 4 * NT * 256 + b * 256);
    }
  };
  auto mm = [&](int a, int b, v4u (&xf)[MT], v2u (&wf)[NTW][2]) {
    const v4u wv = v4u{wf[b][0].x, wf[b][0].y, wf[b][1].x, wf[b][1].y};
    acc[a][b] = TYPE == 1 ? mfma32(xf[a], wv, acc[a][b]) : mfma32(wv, xf[a], acc[a][b]);
  };
  asm volatile("s_waitcnt vmcnt(0)" ::: "memory");
  __builtin_amdgcn_s_barrier();
  STAMP(2);
#pragma unroll
  for (int r = 0; r < NRD; ++r) rd1(r, 0, smem, xa, wa);
  int buf = 0;
  auto body = [&](int kt, auto MODE) {
    constexpr int mode = decltype(MODE)::value;
    const unsigned char* sb = smem + buf * K1_STAGE;
    const int nbuf = buf == 2 ? 0 : buf + 1;
    const unsigned char* sn = smem + nbuf * K1_STAGE;
    __builtin_amdgcn_sched_barrier(0);
#pragma unroll
    for (int i = 0; i < NMM; ++i) {
      mm(i / NTW, i % NTW, xa, wa);
      if (i < NRD) rd1(i, 1, sb, xb, wb);
      __builtin_amdgcn_sched_barrier(0);
    }
    if (mode != 3) {
      asm volatile("s_waitcnt lgkmcnt(0)" ::: "memory");
      __builtin_amdgcn_s_barrier();
    }
    __builtin_amdgcn_sched_barrier(0);
#pragma unroll
    for (int i = 0; i < NMM; ++i) {
      mm(i / NTW, i % NTW, xb, wb);
      if (mode != 3 && i < NRD) rd1(i, 0, sn, xa, wa);
      __builtin_amdgcn_sched_barrier(0);
    }
    buf = nbuf;
  };
#pragma unroll 1
  for (int kt = 0; kt < 9; ++kt) body(kt, std::integral_constant<int, 0>{});
  body(9, std::integral_constant<int, 3>{});
  __builtin_amdgcn_sched_barrier(0);
  STAMP(4);
  __builtin_amdgcn_sched_barrier(0);
  if (TYPE == 0) {
    const int gq = 2 * ct + wn;
#pragma unroll
    for (int a = 0; a < MT; ++a) {
      const int st = (r0 + wm * 48 + 16 * a) >> 4;
      {
        unsigned pk[5][2];
#pragma unroll
        for (int b = 0; b < 5; ++b) {
          pk[b][0] = pack2(phi_f(acc[a][b][0]), phi_f(acc[a][b][1]));
          pk[b][1] = pack2(phi_f(acc[a][b][2]), phi_f(acc[a][b][3]));
        }
        unsigned char* dst = wsb + (st * 16 >= rv ? (unsigned)OFF_PQ + (unsigned)(st * 8 + gq) * 2560u : (unsigned)OFF_DUMP);
        *(v4u*)(dst + lane * 16) = v4u{pk[0][0], pk[0][1], pk[1][0], pk[1][1]};
        *(v4u*)(dst + 1024 + lane * 16) = v4u{pk[2][0], pk[2][1], pk[3][0], pk[3][1]};
        *(v2u*)(dst + 2048 + lane * 8) = v2u{pk[4][0], pk[4][1]};
      }
    }
  } else if (TYPE == 2) {
    f32x2 bias[3][2];
#pragma unroll
    for (int b = 0; b < 3; ++b) {
      const v2u bb = *(const v2u*)(smem + 3 * K1_STAGE + (48 * wn + 16 * b + 4 * g) * 2);
      bias[b][0] = f32x2{bf2f(bb.x & 0xFFFFu), bf2f(bb.x >> 16)};
      bias[b][1] = f32x2{bf2f(bb.y & 0xFFFFu), bf2f(bb.y >> 16)};
    }
    const int ntg = 12 * ct + 3 * wn;
#pragma unroll
    for (int a = 0; a < MT; ++a) {
      const int st = (r0 + wm * 80 + 16 * a) >> 4;
      unsigned pk[3][2];
#pragma unroll
      for (int b = 0; b < 3; ++b)
#pragma unroll
        for (int hf = 0; hf < 2; ++hf) {
          const f32x2 y = gelu2(f32x2{acc[a][b][2 * hf], acc[a][b][2 * hf + 1]} + bias[b][hf]);
          pk[b][hf] = pack2(y.x, y.y);
        }
      unsigned char* dst = wsb + (st * 16 >= rv ? (unsigned)OFF_HF + (unsigned)(st * 54 + (ntg >> 1)) * 1024u : (unsigned)OFF_DUMP) + lane * 16;
      if ((wn & 1) == 0) {
        *(v4u*)dst = v4u{pk[0][0], pk[0][1], pk[1][0], pk[1][1]};
        *(v2u*)(dst + 1024) = v2u{pk[2][0], pk[2][1]};
      } else {
        *(v2u*)(dst + 8) = v2u{pk[0][0], pk[0][1]};
        *(v4u*)(dst + 1024) = v4u{pk[1][0], pk[1][1], pk[2][0], pk[2][1]};
      }
    }
  } else {
    const v2u ones = v2u{0x3F803F80u, 0x3F803F80u};
    v2u fr[3][5];
#pragma unroll
    for (int a = 0; a < 3; ++a) {
      const bool valid = (r0 + wm * 48 + 16 * a) >= rv;
#pragma unroll
      for (int b = 0; b < 5; ++b) {
        const bool isk = (16 * b + li) < 40;
        float v[4];
#pragma unroll
        for (int r = 0; r < 4; ++r) {
          float t = acc[a][b][r];
          if (b < 2) t = phi_f(t);
          else if (b == 2) t = isk ? phi_f(t) : t;
          v[r] = valid ? t : 0.f;
        }
        fr[a][b] = v2u{pack2(v[0], v[1]), pack2(v[2], v[3])};
      }
    }
    __builtin_amdgcn_sched_barrier(0);
    f32x4 d[10];
#pragma unroll
    for (int i = 0; i < 10; ++i) d[i] = f32x4{0.f, 0.f, 0.f, 0.f};
#pragma unroll
    for (int a = 0; a < 3; ++a) {
      d[0] = mfma16(fr[a][0], fr[a][2], d[0]);
      d[1] = mfma16(fr[a][0], fr[a][3], d[1]);
      d[2] = mfma16(fr[a][1], fr[a][2], d[2]);
      d[3] = mfma16(fr[a][1], fr[a][3], d[3]);
      d[4] = mfma16(fr[a][1], fr[a][4], d[4]);
      d[5] = mfma16(fr[a][2], fr[a][3], d[5]);
      d[6] = mfma16(fr[a][2], fr[a][4], d[6]);
      d[7] = mfma16(fr[a][0], ones, d[7]);
      d[8] = mfma16(fr[a][1], ones, d[8]);
      d[9] = mfma16(fr[a][2], ones, d[9]);
    }
    __builtin_amdgcn_sched_barrier(0);
    float* lslab = (float*)(smem + 2 * K1_STAGE) + w * 840;
#pragma unroll
    for (int i = 0; i < 10; ++i) {
      constexpr int N1[10] = {0, 0, 1, 1, 1, 2, 2, 0, 1, 2}, N2[10] = {2, 3, 2, 3, 4, 3, 4, -1, -1, -1};
#pragma unroll
      for (int r = 0; r < 4; ++r) {
        const int c1 = 16 * N1[i] + 4 * g + r, hh = c1 >= 20 ? 1 : 0, dd = c1 - 20 * hh;
        const int e = i < 7 ? 16 * N2[i] + li - 40 - 20 * hh : 20;
        const bool ok = c1 < 40 && (i < 7 ? (e >= 0 && e < 20) : li == 0);
        if (ok) lslab[hh * 420 + dd * 21 + e] = d[i][r];
      }
    }
    __syncthreads();
    if (tid < 420) {
      const int cwn = tid / 210, q = tid % 210;
      const f32x4* src4 = (const f32x4*)(smem + 2 * K1_STAGE) + cwn * 210 + q;
      const f32x4 sum = (src4[0] + src4[420]) + (src4[840] + src4[1260]);
      ((f32x4*)(wsb + OFF_SLAB))[(size_t)(rt * 32 + 4 * ct) * 105 + tid] = sum;
    }
  }
}

template <bool BARS> DI void k1_side(const Params& p, unsigned char* smem, const u16* bias) {
  const int tid = threadIdx.x, lane = tid & 63;
  const int gw = blockIdx.x, g = lane >> 4, li = lane & 15;
  unsigned char* scr = smem + 3 * K1_STAGE + 512;
  const unsigned scr_l = (unsigned)(uintptr_t)(LAS unsigned char*)scr;
  if (BARS) __builtin_amdgcn_s_setprio(3);
#pragma unroll
  for (int it = 0; it < 3; ++it) {
    int id = gw + 256 * it;
    id = id < 740 ? id : 739;
    const int Tt = id / 10, c4 = id % 10;
#pragma unroll
    for (int i = 0; i < 4; ++i) {
      const int j = lane >> 3;
      int row;
      if (Tt < 54) row = 32 * Tt + 16 * (j >> 2) + 4 * i + (j & 3);
      else {
        const int U = Tt - 54, ww = U / 5, u = U % 5, vv = 8 * u + j;
        row = (8 * ww + vv / 5) * 20 + 5 * i + vv % 5;
      }
      glds16((unsigned)(row * D_ + 64 * c4 + 8 * (lane & 7)) * 2u, Tt < 54 ? p.w2 : p.wo, scr_l + it * 4096 + i * 1024);
    }
  }
  v4u xv[2][5], gv[5];
#pragma unroll
  for (int i = 0; i < 5; ++i) {
    gv[i] = *(const v4u*)(p.gw + (i * 16 + li) * 8);
#pragma unroll
    for (int rr = 0; rr < 2; ++rr) xv[rr][i] = *(const v4u*)(p.x + (size_t)(8 * gw + 4 * rr + g) * D_ + (i * 16 + li) * 8);
  }
  unsigned bias2[2] = {0u, 0u};
  if (bias) {
    bias2[0] = ((const unsigned*)bias)[lane];
    bias2[1] = ((const unsigned*)bias)[64 + (lane & 31)];
  }
  if (BARS) {
    __builtin_amdgcn_sched_barrier(0);
#pragma unroll 1
    for (int i = 0; i < 4; ++i) __builtin_amdgcn_s_barrier();
  }
  asm volatile("s_waitcnt vmcnt(0)" ::: "memory");
  __builtin_amdgcn_sched_barrier(0);
  if (bias) {
    ((unsigned*)(smem + 3 * K1_STAGE))[lane] = bias2[0];
    ((unsigned*)(smem + 3 * K1_STAGE))[64 + (lane & 31)] = bias2[1];
  }
#pragma unroll
  for (int it = 0; it < 3; ++it) {
    const int id = gw + 256 * it;
    const int Tt = id / 10, c4 = id % 10;
#pragma unroll
    for (int t4 = 0; t4 < 4; ++t4) {
      const unsigned char* a = scr + it * 4096 + (8 * g + (li >> 2)) * 128 + t4 * 32 + (lane & 3) * 8;
      const v2u lo = tr_read(a), hi = tr_read(a + 512);
      const int ntg = 4 * c4 + t4, nb = ntg / 5, t = ntg % 5;
      const size_t o = Tt < 54 ? OFF_W2F + (size_t)((nb * 54 + Tt) * 5 + t) * 1024 : OFF_WOF + (size_t)((nb * 20 + Tt - 54) * 5 + t) * 1024;
      *(v4u*)(p.ws + (id < 740 ? o : OFF_DUMP) + lane * 16) = v4u{lo.x, lo.y, hi.x, hi.y};
    }
  }
  if (BARS) {
    asm volatile("s_waitcnt lgkmcnt(0)" ::: "memory");
    __builtin_amdgcn_sched_barrier(0);
    __builtin_amdgcn_s_barrier();
    __builtin_amdgcn_sched_barrier(0);
  }
#pragma unroll
  for (int rr = 0; rr < 2; ++rr) {
    float s = 0.f;
#pragma unroll
    for (int i = 0; i < 5; ++i)
#pragma unroll
      for (int q = 0; q < 4; ++q) {
        s += rbf(bf2f(xv[rr][i][q] & 0xFFFFu) * bf2f(gv[i][q] & 0xFFFFu));
        s += rbf(bf2f(xv[rr][i][q] >> 16) * bf2f(gv[i][q] >> 16));
      }
#pragma unroll
    for (int o = 8; o > 0; o >>= 1) s += __shfl_xor(s, o);
    if (li == 0) {
      const float lg = rbf(s);
      const float gg = rbf(1.f / (1.f + __expf(-lg)));
      *(f32x2*)(p.ws + OFF_G + (size_t)(8 * gw + 4 * rr + g) * 8) = f32x2{gg, rbf(1.f - gg)};
    }
    if (BARS && rr == 0) {
      __builtin_amdgcn_sched_barrier(0);
      __builtin_amdgcn_s_barrier();
      __builtin_amdgcn_sched_barrier(0);
    }
  }
  if (blockIdx.x == 0 && lane < 32) *(unsigned*)(p.ws + OFF_CNT + lane * 256) = 0u;
}

__global__ void __launch_bounds__(768, 3) k1_kernel(Params1 p1) {
  extern __shared__ __attribute__((aligned(16))) unsigned char smem[];
  const Params& p = p1.p;
  STAMP_DECL;
  STAMP(0);
  const unsigned t = p1.tm.v[blockIdx.x];
  if (t != 0xFFFFu) {
    const int type = (t >> 12) & 15, rt = (t >> 6) & 63, ct = t & 63;
    if (type == 0) k1_tile<0>(p, smem, rt, ct, t >> 16 STAMP_ARG);
    else if (type == 1) k1_tile<1>(p, smem, rt, ct, t >> 16 STAMP_ARG);
    else k1_tile<2>(p, smem, rt, ct, t >> 16 STAMP_ARG);
  } else {
    const int w = __builtin_amdgcn_readfirstlane(threadIdx.x >> 6);
    if (w == 11) k1_side<false>(p, smem, nullptr);
  }
  STAMP(5);
  if (threadIdx.x < 256) STAMP_STORE(OFF_DBG1, 4);
}

constexpr int AUX_SC1 = 16;
constexpr int RSRC_W3 = 0x00020000;
constexpr int K2_LDS = 4 * 25600;

DI unsigned kvf_pair(const float* slab, int head, int ls, int jp) {
  const int f = ls >> 6, l2 = ls & 63, rho = l2 & 15, g2 = l2 >> 4, hh = head & 3;
  int e;
  if (f == 0) e = 5 * (rho >> 2) + (rho & 3);
  else e = (rho & 3) == 0 ? 5 * (rho >> 2) + 4 : ((rho & 3) == 1 ? 20 : -1);
  float v[2];
#pragma unroll
  for (int q = 0; q < 2; ++q) {
    const int j = 2 * jp + q;
    const int d = 16 * (j >> 2) + 4 * g2 + (j & 3) - 4 * hh;
    const bool ok = (e >= 0) && (d >= 0) && (d < 20);
    const int off = ok ? d * 21 + e : 0;
    float s = 0.f;
#pragma unroll
    for (int pp = 0; pp < 11; ++pp) s += slab[(size_t)(pp * 32 + head) * 420 + off];
    v[q] = ok ? s : 0.f;
  }
  return pack2(v[0], v[1]);
}

__global__ void __launch_bounds__(256, 1) k2_kernel(Params p) {
  extern __shared__ __attribute__((aligned(16))) unsigned char smem[];
  const int tid = threadIdx.x, lane = tid & 63, w = __builtin_amdgcn_readfirstlane(tid >> 6);
  const int g = lane >> 4, li = lane & 15;
  const int b = blockIdx.x, bi = b >> 3;
  const int rb = 4 * (b & 7) + (bi >> 3), nb = bi & 7;
  const __amdgpu_buffer_rsrc_t rs = __builtin_amdgcn_make_buffer_rsrc((void*)p.ws, 0, (int)WS_BYTES, RSRC_W3);
  const float* slab = (const float*)(p.ws + OFF_SLAB);
  GAS unsigned* cnt = (GAS unsigned*)(p.ws + OFF_CNT);
  GAS unsigned* mycnt = cnt + (8 * w + (lane & 7)) * 64;
  STAMP_DECL;
  STAMP(0);
#define K2_DEPTH 3
  constexpr int NSTEP = 14, DEPTH = K2_DEPTH;
  const int T0 = 14 * w;
  const int cntw = w < 3 ? 14 : 12;
  const int vo = lane * 16;
  const int hbase = (int)OFF_HF + (4 * rb * 54) * 1024;
  const int wbase = (int)OFF_W2F + (nb * 54 * 5) * 1024;
  v4u hf[DEPTH][4], wf[DEPTH][5];
  const int wobase = (int)OFF_WOF + ((nb * 20 + 5 * w) * 5) * 1024;
  auto ld_step = [&](int i, v4u (&h)[4], v4u (&ww)[5]) {
    if (i < NSTEP) {
      const int T = T0 + (i < 12 ? i : (i < cntw ? i : cntw - 1));
#pragma unroll
      for (int m = 0; m < 4; ++m) h[m] = __builtin_amdgcn_raw_buffer_load_b128(rs, vo, hbase + (m * 54 + T) * 1024, 0);
#pragma unroll
      for (int t = 0; t < 5; ++t) ww[t] = __builtin_amdgcn_raw_buffer_load_b128(rs, vo, wbase + (T * 5 + t) * 1024, 0);
    } else {
#pragma unroll
      for (int t = 0; t < 5; ++t) ww[t] = __builtin_amdgcn_raw_buffer_load_b128(rs, vo, wobase + ((i - NSTEP) * 5 + t) * 1024, 0);
    }
  };
  if (w == 3) {
    const int head = b >> 3, ls = (b & 7) * 16 + (lane >> 2), jp = lane & 3;
    const unsigned v = kvf_pair(slab, head, ls, jp);
    __builtin_amdgcn_raw_buffer_store_b32(v, rs, (int)OFF_KVF + head * 2048 + ls * 16 + jp * 4, 0, AUX_SC1);
    asm volatile("s_waitcnt vmcnt(0)" ::: "memory");
    if (lane == 0) __hip_atomic_fetch_add(cnt + head * 64, 1u, __ATOMIC_RELAXED, __HIP_MEMORY_SCOPE_AGENT);
  }
  __builtin_amdgcn_sched_barrier(0);
#pragma unroll
  for (int i = 0; i < DEPTH; ++i) ld_step(i, hf[i], wf[i]);
  f32x2 gv[4];
#pragma unroll
  for (int m = 0; m < 4; ++m) gv[m] = *(const f32x2*)(p.ws + OFF_G + (size_t)(64 * rb + 16 * m + li) * 8);
  __builtin_amdgcn_sched_barrier(0);
  __builtin_amdgcn_sched_barrier(0);
  STAMP(1);
  f32x4 acc[5][4];
#pragma unroll
  for (int t = 0; t < 5; ++t)
#pragma unroll
    for (int m = 0; m < 4; ++m) acc[t][m] = f32x4{0.f, 0.f, 0.f, 0.f};
  v4u pa[4], pb[4], kvf[4][2], of[4][5];
  v2u pc[4];
  unsigned pollv = 0;
  int kvbase = (int)OFF_KVF + 8 * w * 2048;
  auto ld_half = [&](int gr) {
#pragma unroll
    for (int hh = 0; hh < 4; ++hh)
#pragma unroll
      for (int f = 0; f < 2; ++f)
        kvf[hh][f] = __builtin_amdgcn_raw_buffer_load_b128(rs, vo, kvbase + (4 * gr + hh) * 2048 + f * 1024, AUX_SC1);
#pragma unroll
    for (int m = 0; m < 4; ++m) {
      const int o = (int)OFF_PQ + ((4 * rb + m) * 8 + 2 * w + gr) * 2560;
      pa[m] = __builtin_amdgcn_raw_buffer_load_b128(rs, vo, o, 0);
      pb[m] = __builtin_amdgcn_raw_buffer_load_b128(rs, vo, o + 1024, 0);
      pc[m] = __builtin_amdgcn_raw_buffer_load_b64(rs, lane * 8, o + 2048, 0);
    }
  };
  auto o_calc = [&](int m, int gr) {
    float ov[20];
#pragma unroll
    for (int hh = 0; hh < 4; ++hh) {
      v4u bf;
      if (hh == 0) bf = pa[m];
      else if (hh == 1) bf = v4u{pa[m].z, pa[m].w, pb[m].x, pb[m].y};
      else if (hh == 2) bf = pb[m];
      else bf = v4u{pb[m].z, pb[m].w, pc[m].x, pc[m].y};
      const f32x4 z4 = f32x4{0.f, 0.f, 0.f, 0.f};
      const f32x4 c0 = mfma32(kvf[hh][0], bf, z4);
      const f32x4 c1 = mfma32(kvf[hh][1], bf, z4);
      const float sc = gv[m].x * __builtin_amdgcn_rcpf(c1[1] + 1e-6f);
      ov[5 * hh + 0] = c0[0] * sc;
      ov[5 * hh + 1] = c0[1] * sc;
      ov[5 * hh + 2] = c0[2] * sc;
      ov[5 * hh + 3] = c0[3] * sc;
      ov[5 * hh + 4] = c1[0] * sc;
    }
    if (gr == 0) {
      of[m][0] = v4u{pack2(ov[0], ov[1]), pack2(ov[2], ov[3]), pack2(ov[4], ov[5]), pack2(ov[6], ov[7])};
      of[m][1] = v4u{pack2(ov[8], ov[9]), pack2(ov[10], ov[11]), pack2(ov[12], ov[13]), pack2(ov[14], ov[15])};
      of[m][2].x = pack2(ov[16], ov[17]);
      of[m][2].y = pack2(ov[18], ov[19]);
    } else {
      of[m][2].z = pack2(ov[0], ov[1]);
      of[m][2].w = pack2(ov[2], ov[3]);
      of[m][3] = v4u{pack2(ov[4], ov[5]), pack2(ov[6], ov[7]), pack2(ov[8], ov[9]), pack2(ov[10], ov[11])};
      of[m][4] = v4u{pack2(ov[12], ov[13]), pack2(ov[14], ov[15]), pack2(ov[16], ov[17]), pack2(ov[18], ov[19])};
    }
  };
  static_for<NSTEP + 5>([&](auto IC) {
    constexpr int i = decltype(IC)::value;
    if (i == NSTEP - 1 || i == NSTEP - 2) {
      const unsigned msk = cntw == 14 ? 0xFFFFFFFFu : 0u;
#pragma unroll
      for (int m = 0; m < 4; ++m) hf[i % DEPTH][m] &= v4u{msk, msk, msk, msk};
    }
    if (i == NSTEP) {
#pragma unroll
      for (int t = 0; t < 5; ++t)
#pragma unroll
        for (int m = 0; m < 4; ++m) acc[t][m] *= gv[m].y;
      __builtin_amdgcn_sched_barrier(0);
    }
#pragma unroll
    for (int t = 0; t < 5; ++t)
#pragma unroll
      for (int m = 0; m < 4; ++m) {
        acc[t][m] = mfma32(wf[i % DEPTH][t], i < NSTEP ? hf[i % DEPTH][m] : of[m][i < NSTEP ? 0 : i - NSTEP], acc[t][m]);
      }
    if (i + DEPTH < NSTEP + 5) ld_step(i + DEPTH, hf[i % DEPTH], wf[i % DEPTH]);
    if (i == 8) pollv = __hip_atomic_load(mycnt, __ATOMIC_RELAXED, __HIP_MEMORY_SCOPE_AGENT);
    if (i == 10) {
      bool ok = __all(pollv == 8u);
      for (int spins = 0; !ok && spins < 200000; ++spins) {
        __builtin_amdgcn_s_sleep(16);
        const unsigned c = __hip_atomic_load(mycnt, __ATOMIC_RELAXED, __HIP_MEMORY_SCOPE_AGENT);
        ok = __all(c == 8u);
      }
      __builtin_amdgcn_fence(__ATOMIC_ACQUIRE, "wavefront");
      if (!ok) {
        const int fb = (int)OFF_FB + b * 65536 + 8 * w * 2048;
        for (int it = 0; it < 8 * 8; ++it) {
          const int hl = it >> 3, ls = (it & 7) * 16 + (lane >> 2), jp = lane & 3;
          const unsigned v = kvf_pair(slab, 8 * w + hl, ls, jp);
          __builtin_amdgcn_raw_buffer_store_b32(v, rs, fb + hl * 2048 + ls * 16 + jp * 4, 0, AUX_SC1);
        }
        asm volatile("s_waitcnt vmcnt(0)" ::: "memory");
        kvbase = fb;
      }
      ld_half(0);
    }
    if (i == 12) {
#pragma unroll
      for (int m = 0; m < 4; ++m) { __builtin_amdgcn_sched_barrier(0); o_calc(m, 0); __builtin_amdgcn_sched_barrier(0); }
      ld_half(1);
    }
    if (i == 14) {
#pragma unroll
      for (int m = 0; m < 4; ++m) { __builtin_amdgcn_sched_barrier(0); o_calc(m, 1); __builtin_amdgcn_sched_barrier(0); }
    }
    __builtin_amdgcn_sched_barrier(0);
  });
  __builtin_amdgcn_sched_barrier(0);
  f32x4* red = (f32x4*)smem;
#pragma unroll
  for (int t = 0; t < 5; ++t)
#pragma unroll
    for (int m = 0; m < 4; ++m) red[w * 1600 + (m * 5 + t) * 64 + lane] = acc[t][m];
  v2u bb[5];
#pragma unroll
  for (int t = 0; t < 5; ++t) bb[t] = *(const v2u*)(p.b2 + 80 * nb + 16 * t + 4 * g);
  __syncthreads();
  const float omg = w == 0 ? gv[0].y : (w == 1 ? gv[1].y : (w == 2 ? gv[2].y : gv[3].y));
  u16* orow = p.out + (size_t)(64 * rb + 16 * w + li) * D_ + 80 * nb + 4 * g;
#pragma unroll
  for (int t = 0; t < 5; ++t) {
    f32x4 s = red[(w * 5 + t) * 64 + lane];
#pragma unroll
    for (int ww = 1; ww < 4; ++ww) s += red[ww * 1600 + (w * 5 + t) * 64 + lane];
    const float o0 = s[0] + omg * bf2f(bb[t].x & 0xFFFFu), o1 = s[1] + omg * bf2f(bb[t].x >> 16);
    const float o2 = s[2] + omg * bf2f(bb[t].y & 0xFFFFu), o3 = s[3] + omg * bf2f(bb[t].y >> 16);
    *(v2u*)(orow + 16 * t) = v2u{pack2(o0, o1), pack2(o2, o3)};
  }
  STAMP(6);
  STAMP_STORE(OFF_DBG2, 4);
}

}

extern "C" void kernel_launch(void* const* d_in, const int* in_sizes, int n_in, void* d_out, int out_size, void* d_ws,
                              size_t ws_size, hipStream_t stream) {
  Params p;
  p.x = (const u16*)d_in[0];
  p.wqkv = (const u16*)d_in[1];
  p.wo = (const u16*)d_in[2];
  p.w1 = (const u16*)d_in[4];
  p.b1 = (const u16*)d_in[5];
  p.w2 = (const u16*)d_in[6];
  p.b2 = (const u16*)d_in[7];
  p.gw = (const u16*)d_in[8];
  p.out = (u16*)d_out;
  p.ws = (unsigned char*)d_ws;
  (void)hipFuncSetAttribute((const void*)k1_kernel, hipFuncAttributeMaxDynamicSharedMemorySize, K1_LDS);
  (void)hipFuncSetAttribute((const void*)k2_kernel, hipFuncAttributeMaxDynamicSharedMemorySize, K2_LDS);
  Params1 p1;
  p1.p = p;
  constexpr TileMap tm = make_tilemap();
  p1.tm = tm;
  k1_kernel<<<256, 768, K1_LDS, stream>>>(p1);
  k2_kernel<<<256, 256, K2_LDS, stream>>>(p);
}
```

```cpp
#include <hip/hip_runtime.h>
#include <hip/hip_bf16.h>
#include <stdint.h>
#include <type_traits>
#include <utility>

namespace {

constexpr int S_ = 2048, D_ = 640, F_ = 1728;
typedef unsigned short u16;
typedef unsigned v4u __attribute__((ext_vector_type(4)));
typedef unsigned v2u __attribute__((ext_vector_type(2)));
typedef float f32x4 __attribute__((ext_vector_type(4)));
typedef float f32x2 __attribute__((ext_vector_type(2)));
typedef short bf16x8 __attribute__((ext_vector_type(8)));
typedef short bf16x4 __attribute__((ext_vector_type(4)));
typedef short v4i16_t __attribute__((ext_vector_type(4)));
typedef __bf16 bf2_t __attribute__((ext_vector_type(2)));
#define LAS __attribute__((address_space(3)))
#define GAS __attribute__((address_space(1)))
#define DI __device__ __forceinline__

constexpr size_t OFF_CNT = 0;
constexpr size_t OFF_G = 32 * 256;
constexpr size_t OFF_KVF = OFF_G + 2048 * 8;
constexpr size_t OFF_SLAB = OFF_KVF + 65536;
constexpr size_t SLAB_BYTES = ((size_t)11 * 32 * 420 * 4 + 255) / 256 * 256;
constexpr size_t OFF_W2F = OFF_SLAB + SLAB_BYTES;
constexpr size_t OFF_WOF = OFF_W2F + (size_t)8 * 54 * 5 * 1024;
constexpr size_t OFF_PQ = OFF_WOF + (size_t)8 * 20 * 5 * 1024;
constexpr size_t OFF_HF = OFF_PQ + (size_t)128 * 8 * 2560;
constexpr size_t OFF_FB = OFF_HF + (size_t)128 * 54 * 1024;
constexpr size_t OFF_DUMP = OFF_FB + (size_t)256 * 65536;
constexpr size_t WS_BYTES = OFF_DUMP + 65536;
static_assert(OFF_SLAB % 256 == 0 && OFF_W2F % 256 == 0 && OFF_PQ % 256 == 0 && OFF_HF % 256 == 0, "align");
static_assert(WS_BYTES < (1ull << 31), "int offsets");

struct Params {
  const u16* x;
  const u16* wqkv;
  const u16* wo;
  const u16* w1;
  const u16* b1;
  const u16* w2;
  const u16* b2;
  const u16* gw;
  u16* out;
  unsigned char* ws;
};

template <int... I, class Fn> DI void static_for_impl(std::integer_sequence<int, I...>, Fn&& fn) { (fn(std::integral_constant<int, I>{}), ...); }
template <int N, class Fn> DI void static_for(Fn&& fn) { static_for_impl(std::make_integer_sequence<int, N>{}, fn); }
DI float bf2f(unsigned v) { return __builtin_bit_cast(float, v << 16); }
DI float rbf(float a) { return (float)(__bf16)a; }
DI unsigned pack2(float a, float b) {
  f32x2 v = {a, b};
  bf2_t r = __builtin_convertvector(v, bf2_t);
  return __builtin_bit_cast(unsigned, r);
}
DI float phi_f(float t) {
  const float e = __builtin_amdgcn_exp2f(t * 1.4426950408889634f);
  return (t > 0.f ? t + 1.f : e) + 1e-6f;
}
DI f32x2 gelu2(f32x2 x) {
  const f32x2 c1 = {-2.302208198f, -2.302208198f}, c2 = {-0.1029432397f, -0.1029432397f};
  const f32x2 e = x * (x * x * c2 + c1);
  const f32x2 d = f32x2{__builtin_amdgcn_exp2f(e.x), __builtin_amdgcn_exp2f(e.y)} + f32x2{1.f, 1.f};
  return x * f32x2{__builtin_amdgcn_rcpf(d.x), __builtin_amdgcn_rcpf(d.y)};
}
DI f32x4 mfma32(v4u a, v4u b, f32x4 c) {
  return __builtin_amdgcn_mfma_f32_16x16x32_bf16(__builtin_bit_cast(bf16x8, a), __builtin_bit_cast(bf16x8, b), c, 0, 0, 0);
}
DI f32x4 mfma16(v2u a, v2u b, f32x4 c) {
  return __builtin_amdgcn_mfma_f32_16x16x16bf16_1k(__builtin_bit_cast(bf16x4, a), __builtin_bit_cast(bf16x4, b), c, 0, 0, 0);
}
DI v2u tr_read(const unsigned char* p) {
  return __builtin_bit_cast(v2u, __builtin_amdgcn_ds_read_tr16_b64_v4i16((LAS v4i16_t*)p));
}
DI void glds16(unsigned voff, const void* sbase, unsigned lds_dst) {
  unsigned keep;
  asm volatile("s_mov_b32 %0, m0\n\ts_mov_b32 m0, %3\n\ts_nop 0\n\tglobal_load_lds_dwordx4 %1, %2\n\ts_mov_b32 m0, %0"
               : "=&s"(keep) : "v"(voff), "s"(sbase), "s"(lds_dst) : "memory");
}
#define STAMP(i)
#define STAMP_DECL
#define STAMP_PARAM
#define STAMP_ARG
#define STAMP_STORE(base, nw)
constexpr size_t OFF_DBG1 = (size_t)64 << 20, OFF_DBG2 = (size_t)65 << 20;

struct TileMap { unsigned v[256]; };
constexpr TileMap make_tilemap() {
  TileMap m{};
  for (int i = 0; i < 256; ++i) m.v[i] = 0xFFFF;
  for (int half = 0; half < 2; ++half) {
    const int q0 = half ? 6 : 0, q1 = half ? 11 : 6, h0 = half ? 6 : 0, h1 = half ? 13 : 6;
    for (int xq = 0; xq < 4; ++xq) {
      int n = 0;
      const int x = half * 4 + xq;
      for (int c = 0; c < 2; ++c)
        for (int rt = q0; rt < q1; ++rt) m.v[x + 8 * n++] = (unsigned)((1 << 12) | (rt << 6) | (2 * xq + c));
      for (int rt = q0; rt < q1; ++rt) m.v[x + 8 * n++] = (unsigned)((0 << 12) | (rt << 6) | xq);
      for (int c = 0; c < 2; ++c)
        for (int rt = h0; rt < h1; ++rt) m.v[x + 8 * n++] = (unsigned)((2 << 12) | (rt << 6) | (2 * xq + c));
      for (int rt = h0; rt < h1; ++rt)
        if (((rt - h0) & 3) == xq) m.v[x + 8 * n++] = (unsigned)((2 << 12) | (rt << 6) | 8);
    }
  }
  for (int b = 0; b < 256; ++b) {
    const unsigned t = m.v[b] & 0xFFFF;
    if (t == 0xFFFF) continue;
    int rr = 0, rc = 0, cr = 0, cc = 0;
    for (int o = b & 7; o < 256; o += 8) {
      const unsigned u = m.v[o] & 0xFFFF;
      if (u == 0xFFFF) continue;
      if (((u >> 12) == 2) == ((t >> 12) == 2) && ((u >> 6) & 63) == ((t >> 6) & 63)) { if (o < b) ++rr; ++rc; }
      if ((u >> 12) == (t >> 12) && (u & 63) == (t & 63)) { if (o < b) ++cr; ++cc; }
    }
    m.v[b] = t | (unsigned)(rr | rc << 4) << 16 | (unsigned)(cr | cc << 4) << 24;
  }
  return m;
}
struct Params1 {
  Params p;
  TileMap tm;
};

#define TOUCH0 3
constexpr int CODE1_KB = 40, CODE2_LINES = 116;
constexpr int K1_STAGE = 45056;
constexpr int K1_LDS = 3 * K1_STAGE + 512 + 16384;

DI void k1_side(const Params& p, unsigned char* smem, int w, const u16* bias);

template <int TYPE>
DI void k1_tile(const Params& p, unsigned char* smem, int rt, int ct, unsigned share STAMP_PARAM) {
  constexpr int MT = TYPE == 2 ? 5 : 3, NTW = TYPE == 2 ? 3 : 5, WM = TYPE == 2 ? 2 : 4, WN = TYPE == 2 ? 4 : 2;
  constexpr int BM = MT * 16 * WM, BN = NTW * 16 * WN, NT = BN / 16, XB = BM * 128, NMM = MT * NTW;
  constexpr int NX = BM / 32, NW = NT / 2;
  constexpr int NRD = MT + 2 * NTW;
  static_assert(XB + 64 * BN * 2 == K1_STAGE && NX + NW == 11, "stage");
  const int tid = threadIdx.x, lane = tid & 63, w = __builtin_amdgcn_readfirstlane(tid >> 6);
  const int wm = w / WN, wn = w % WN, g = lane >> 4, li = lane & 15;
  if (w >= 10) {
    k1_side(p, smem, w - 10, TYPE == 2 ? p.b1 + 192 * ct : nullptr);
    return;
  }
  const int rv = rt * BM, r0 = rv + BM <= S_ ? rv : S_ - BM;
  const u16* W = TYPE == 2 ? p.w1 : p.wqkv;
  constexpr int LDW = TYPE == 2 ? F_ : 3 * D_;
  const unsigned srcx = (unsigned)((r0 + (lane >> 3)) * D_ + 8 * ((lane & 7) ^ (lane >> 3))) * 2u;
  unsigned src[NW];
#pragma unroll
  for (int i = 0; i < NW; ++i) {
    const int cc = 64 * i + lane;
    const int kbl = cc / (16 * NT), c2 = cc % (16 * NT);
    const int nt = c2 >> 4, rowp = (c2 >> 1) & 7, half = c2 & 1;
    const int k = 8 * kbl + (rowp ^ (4 * kbl));
    const int col = 16 * nt + 8 * half;
    int gcol;
    if (TYPE == 0) gcol = 160 * ct + col;
    else if (TYPE == 1) {
      gcol = (col < 80 ? D_ + col : 2 * D_ + col - 80) + 80 * ct;
    } else gcol = 192 * ct + col;
    src[i] = (unsigned)(k * LDW + gcol) * 2u;
  }
  const unsigned lds0 = (unsigned)(uintptr_t)(LAS unsigned char*)smem;
  constexpr int WB = K1_STAGE - XB;
  auto issue1 = [&](int i, int wv, int kt, int st) {
    if (i < NX) glds16(srcx, p.x + kt * 64 + (wv * (BM / 4) + i * 8) * D_, lds0 + st * XB + (wv * BM * 2 + 64 * i) * 16);
    else glds16(src[i - NX], W + (size_t)(kt * 64 + wv * 16) * LDW, lds0 + 3 * XB + st * WB + (wv * 32 * NT + 64 * (i - NX)) * 16);
  };
  if (w >= 8) {
    const int q0 = 2 * (w - 8);
#pragma unroll
    for (int t = 1; t < 3; ++t)
#pragma unroll
      for (int i = 0; i < 22; ++i) issue1(i % 11, q0 + i / 11, t, t);
    __builtin_amdgcn_s_barrier();
    int bf = 0;
#pragma unroll 1
    for (int kt = 0; kt < 9; ++kt) {
      if (kt < 8) asm volatile("s_waitcnt vmcnt(22)" ::: "memory");
      else asm volatile("s_waitcnt vmcnt(0)" ::: "memory");
      __builtin_amdgcn_s_barrier();
      if (kt + 3 < 10) {
#pragma unroll
        for (int i = 0; i < 22; ++i) issue1(i % 11, q0 + i / 11, kt + 3, bf);
      }
      bf = bf == 2 ? 0 : bf + 1;
    }
    return;
  }
#pragma unroll
  for (int i = 0; i < 11; ++i)
    if ((i & 1) == (w & 1)) issue1(i, w >> 1, 0, 0);
  STAMP(1);
  const unsigned xoff0 = (unsigned)((wm * MT * 16 + li) * 128 + ((g ^ (lane & 7)) << 4));
  const unsigned woff0 = (unsigned)(3 * XB + g * NT * 256 + wn * NTW * 256 + (4 * (g & 1) + (li >> 2)) * 32 + (lane & 3) * 8);
  unsigned woffk[5];
#pragma unroll
  for (int b = 0; b < 5; ++b) {
    const int cc = 16 * b + 4 * (lane & 3);
    const int col = cc < 40 ? 40 * wn + cc : 40 + 40 * wn + cc;
    woffk[b] = (unsigned)(3 * XB + g * NT * 256 + (col >> 4) * 256 + (4 * (g & 1) + (li >> 2)) * 32 + (col & 15) * 2);
  }
  auto raw_tile = [&](int ww, int i) {
    return (f32x4*)(smem + (ww < 4 ? XB + ww * 10240 : 3 * XB + WB + (ww - 4) * 10240) + i * 1024);
  };
  unsigned goff[2] = {0u, 0u};
  if (TYPE == 1) {
#pragma unroll
    for (int q = 0; q < 4; ++q) {
      const int o = (4 * tid + q) % 840, hh = o / 420, rem = o % 420, dd = rem / 21, e = rem % 21;
      const int c1 = 20 * hh + dd, n1 = c1 >> 4, row = c1 & 15;
      const int c2 = 40 + 20 * hh + e, n2 = c2 >> 4;
      const int tile = e < 20 ? (n1 == 0 ? n2 - 2 : (n1 == 1 ? n2 : n2 + 2)) : 7 + n1;
      const int col = e < 20 ? (c2 & 15) : 0;
      goff[q >> 1] |= (unsigned)(tile * 256 + ((row >> 2) * 16 + col) * 4 + (row & 3)) << (16 * (q & 1));
    }
    asm volatile("" : "+v"(goff[0]), "+v"(goff[1]));
  }
  unsigned char* wsb = p.ws;
  asm volatile("" : "+s"(wsb));
  f32x4 acc[MT][NTW];
#pragma unroll
  for (int a = 0; a < MT; ++a)
#pragma unroll
    for (int b = 0; b < NTW; ++b) acc[a][b] = f32x4{0.f, 0.f, 0.f, 0.f};
  v4u xa[MT], xb[MT];
  v2u wa[NTW][2], wb[NTW][2];
  auto rd1 = [&](int r, int ks, int st, v4u (&xf)[MT], v2u (&wf)[NTW][2]) {
    if (r < MT) xf[r] = *(const v4u*)(smem + (xoff0 ^ (ks * 64)) + (st * XB + r * 2048));
    else {
      const int b = (r - MT) >> 1, hi = (r - MT) & 1;
      if (TYPE == 1) wf[b][hi] = tr_read(smem + (woffk[b < 5 ? b : 0] ^ (hi * 128)) + (st * WB + ks * 4 * NT * 256));
      else wf[b][hi] = tr_read(smem + (woff0 ^ (hi * 128)) + (st * WB + ks * 4 * NT * 256 + b * 256));
    }
  };
  auto mm = [&](int a, int b, v4u (&xf)[MT], v2u (&wf)[NTW][2]) {
    const v4u wv = v4u{wf[b][0].x, wf[b][0].y, wf[b][1].x, wf[b][1].y};
    acc[a][b] = TYPE == 1 ? mfma32(xf[a], wv, acc[a][b]) : mfma32(wv, xf[a], acc[a][b]);
  };
  asm volatile("s_waitcnt vmcnt(0)" ::: "memory");
  __builtin_amdgcn_s_barrier();
  STAMP(2);
#pragma unroll
  for (int r = 0; r < NRD; ++r) rd1(r, 0, 0, xa, wa);
  auto body = [&](int kt, auto MODE, auto BUF) {
    constexpr int mode = decltype(MODE)::value, sb = decltype(BUF)::value, sn = (sb + 1) % 3;
    __builtin_amdgcn_sched_barrier(0);
#pragma unroll
    for (int i = 0; i < NMM; ++i) {
      mm(i / NTW, i % NTW, xa, wa);
      if (i < NRD) rd1(i, 1, sb, xb, wb);
      __builtin_amdgcn_sched_barrier(0);
    }
    if (mode != 3) {
      asm volatile("s_waitcnt lgkmcnt(0)" ::: "memory");
      __builtin_amdgcn_s_barrier();
    }
    __builtin_amdgcn_sched_barrier(0);
#pragma unroll
    for (int i = 0; i < NMM; ++i) {
      mm(i / NTW, i % NTW, xb, wb);
      if (mode != 3 && i < NRD) rd1(i, 0, sn, xa, wa);
      __builtin_amdgcn_sched_barrier(0);
    }
  };
#pragma unroll 1
  for (int kt = 0; kt < 9; kt += 3) {
    body(kt, std::integral_constant<int, 0>{}, std::integral_constant<int, 0>{});
    body(kt + 1, std::integral_constant<int, 0>{}, std::integral_constant<int, 1>{});
    body(kt + 2, std::integral_constant<int, 0>{}, std::integral_constant<int, 2>{});
  }
  body(9, std::integral_constant<int, 3>{}, std::integral_constant<int, 0>{});
  __builtin_amdgcn_sched_barrier(0);
  STAMP(4);
  __builtin_amdgcn_sched_barrier(0);
  if (TYPE == 0) {
    const int gq = 2 * ct + wn;
#pragma unroll
    for (int a = 0; a < MT; ++a) {
      const int st = (r0 + wm * 48 + 16 * a) >> 4;
      {
        unsigned pk[5][2];
#pragma unroll
        for (int b = 0; b < 5; ++b) {
          pk[b][0] = pack2(phi_f(acc[a][b][0]), phi_f(acc[a][b][1]));
          pk[b][1] = pack2(phi_f(acc[a][b][2]), phi_f(acc[a][b][3]));
        }
        unsigned char* dst = wsb + (st * 16 >= rv ? (unsigned)OFF_PQ + (unsigned)(st * 8 + gq) * 2560u : (unsigned)OFF_DUMP);
        *(v4u*)(dst + lane * 16) = v4u{pk[0][0], pk[0][1], pk[1][0], pk[1][1]};
        *(v4u*)(dst + 1024 + lane * 16) = v4u{pk[2][0], pk[2][1], pk[3][0], pk[3][1]};
        *(v2u*)(dst + 2048 + lane * 8) = v2u{pk[4][0], pk[4][1]};
      }
    }
  } else if (TYPE == 2) {
    f32x2 bias[3][2];
#pragma unroll
    for (int b = 0; b < 3; ++b) {
      const v2u bb = *(const v2u*)(smem + 3 * K1_STAGE + (48 * wn + 16 * b + 4 * g) * 2);
      bias[b][0] = f32x2{bf2f(bb.x & 0xFFFFu), bf2f(bb.x >> 16)};
      bias[b][1] = f32x2{bf2f(bb.y & 0xFFFFu), bf2f(bb.y >> 16)};
    }
    const int ntg = 12 * ct + 3 * wn;
#pragma unroll
    for (int a = 0; a < MT; ++a) {
      const int st = (r0 + wm * 80 + 16 * a) >> 4;
      unsigned pk[3][2];
#pragma unroll
      for (int b = 0; b < 3; ++b)
#pragma unroll
        for (int hf = 0; hf < 2; ++hf) {
          const f32x2 y = gelu2(f32x2{acc[a][b][2 * hf], acc[a][b][2 * hf + 1]} + bias[b][hf]);
          pk[b][hf] = pack2(y.x, y.y);
        }
      unsigned char* dst = wsb + (st * 16 >= rv ? (unsigned)OFF_HF + (unsigned)(st * 54 + (ntg >> 1)) * 1024u : (unsigned)OFF_DUMP) + lane * 16;
      if ((wn & 1) == 0) {
        *(v4u*)dst = v4u{pk[0][0], pk[0][1], pk[1][0], pk[1][1]};
        *(v2u*)(dst + 1024) = v2u{pk[2][0], pk[2][1]};
      } else {
        *(v2u*)(dst + 8) = v2u{pk[0][0], pk[0][1]};
        *(v4u*)(dst + 1024) = v4u{pk[1][0], pk[1][1], pk[2][0], pk[2][1]};
      }
    }
  } else {
    const v2u ones = v2u{0x3F803F80u, 0x3F803F80u};
    v2u fr[3][5];
#pragma unroll
    for (int a = 0; a < 3; ++a) {
      const bool valid = (r0 + wm * 48 + 16 * a) >= rv;
#pragma unroll
      for (int b = 0; b < 5; ++b) {
        const bool isk = (16 * b + li) < 40;
        float v[4];
#pragma unroll
        for (int r = 0; r < 4; ++r) {
          float t = acc[a][b][r];
          if (b < 2) t = phi_f(t);
          else if (b == 2) t = isk ? phi_f(t) : t;
          v[r] = valid ? t : 0.f;
        }
        fr[a][b] = v2u{pack2(v[0], v[1]), pack2(v[2], v[3])};
      }
    }
    __builtin_amdgcn_sched_barrier(0);
    f32x4 d[10];
#pragma unroll
    for (int i = 0; i < 10; ++i) d[i] = f32x4{0.f, 0.f, 0.f, 0.f};
#pragma unroll
    for (int a = 0; a < 3; ++a) {
      d[0] = mfma16(fr[a][0], fr[a][2], d[0]);
      d[1] = mfma16(fr[a][0], fr[a][3], d[1]);
      d[2] = mfma16(fr[a][1], fr[a][2], d[2]);
      d[3] = mfma16(fr[a][1], fr[a][3], d[3]);
      d[4] = mfma16(fr[a][1], fr[a][4], d[4]);
      d[5] = mfma16(fr[a][2], fr[a][3], d[5]);
      d[6] = mfma16(fr[a][2], fr[a][4], d[6]);
      d[7] = mfma16(fr[a][0], ones, d[7]);
      d[8] = mfma16(fr[a][1], ones, d[8]);
      d[9] = mfma16(fr[a][2], ones, d[9]);
    }
    __builtin_amdgcn_sched_barrier(0);
    f32x4* raw = (f32x4*)(smem + XB);
#pragma unroll
    for (int i = 0; i < 10; ++i) raw_tile(w, i)[lane] = d[i];
    __syncthreads();
    if (tid < 420) {
      const int cwn = tid / 210;
      f32x4 sum;
#pragma unroll
      for (int q = 0; q < 4; ++q) {
        const unsigned off = (goff[q >> 1] >> (16 * (q & 1))) & 0xFFFFu;
        float a = 0.f;
#pragma unroll
        for (int m = 0; m < 4; ++m) a += ((const float*)raw_tile(2 * m + cwn, 0))[off];
        sum[q] = a;
      }
      ((f32x4*)(wsb + OFF_SLAB))[(size_t)(rt * 32 + 4 * ct) * 105 + tid] = sum;
    }
    (void)raw;
  }
}

DI void k1_side(const Params& p, unsigned char* smem, int w, const u16* bias) {
  const int tid = threadIdx.x, lane = tid & 63;
  const int gw = blockIdx.x * 2 + w, g = lane >> 4, li = lane & 15;
  unsigned char* scr = smem + 3 * K1_STAGE + 512 + w * 8192;
  const unsigned scr_l = (unsigned)(uintptr_t)(LAS unsigned char*)scr;
#pragma unroll
  for (int it = 0; it < 2; ++it) {
    int id = gw + 512 * it;
    id = id < 740 ? id : 739;
    const int Tt = id / 10, c4 = id % 10;
#pragma unroll
    for (int i = 0; i < 4; ++i) {
      const int j = lane >> 3;
      int row;
      if (Tt < 54) row = 32 * Tt + 16 * (j >> 2) + 4 * i + (j & 3);
      else {
        const int U = Tt - 54, ww = U / 5, u = U % 5, vv = 8 * u + j;
        row = (8 * ww + vv / 5) * 20 + 5 * i + vv % 5;
      }
      glds16((unsigned)(row * D_ + 64 * c4 + 8 * (lane & 7)) * 2u, Tt < 54 ? p.w2 : p.wo, scr_l + it * 4096 + i * 1024);
    }
  }
  v4u xv[5], gv[5];
#pragma unroll
  for (int i = 0; i < 5; ++i) {
    gv[i] = *(const v4u*)(p.gw + (i * 16 + li) * 8);
    xv[i] = *(const v4u*)(p.x + (size_t)(4 * gw + g) * D_ + (i * 16 + li) * 8);
  }
  unsigned bias2 = 0u;
  if (bias) bias2 = ((const unsigned*)bias)[w * 48 + (lane < 48 ? lane : 47)];
  asm volatile("s_waitcnt vmcnt(0)" ::: "memory");
  __builtin_amdgcn_sched_barrier(0);
  if (bias && lane < 48) ((unsigned*)(smem + 3 * K1_STAGE))[w * 48 + lane] = bias2;
#pragma unroll
  for (int it = 0; it < 2; ++it) {
    const int id = gw + 512 * it;
    const int Tt = id / 10, c4 = id % 10;
#pragma unroll
    for (int t4 = 0; t4 < 4; ++t4) {
      const unsigned char* a = scr + it * 4096 + (8 * g + (li >> 2)) * 128 + t4 * 32 + (lane & 3) * 8;
      const v2u lo = tr_read(a), hi = tr_read(a + 512);
      const int ntg = 4 * c4 + t4, nb = ntg / 5, t = ntg % 5;
      const size_t o = Tt < 54 ? OFF_W2F + (size_t)((nb * 54 + Tt) * 5 + t) * 1024 : OFF_WOF + (size_t)((nb * 20 + Tt - 54) * 5 + t) * 1024;
      *(v4u*)(p.ws + (id < 740 ? o : OFF_DUMP) + lane * 16) = v4u{lo.x, lo.y, hi.x, hi.y};
    }
  }
  {
    float s = 0.f;
#pragma unroll
    for (int i = 0; i < 5; ++i)
#pragma unroll
      for (int q = 0; q < 4; ++q) {
        s = __builtin_fmaf(bf2f(xv[i][q] & 0xFFFFu), bf2f(gv[i][q] & 0xFFFFu), s);
        s = __builtin_fmaf(__builtin_bit_cast(float, xv[i][q] & 0xFFFF0000u), __builtin_bit_cast(float, gv[i][q] & 0xFFFF0000u), s);
      }
#pragma unroll
    for (int o = 8; o > 0; o >>= 1) s += __shfl_xor(s, o);
    if (li == 0) {
      const float lg = rbf(s);
      const float gg = rbf(1.f / (1.f + __expf(-lg)));
      *(f32x2*)(p.ws + OFF_G + (size_t)(4 * gw + g) * 8) = f32x2{gg, rbf(1.f - gg)};
    }
  }
  if (blockIdx.x == 0 && w == 0 && lane < 32) *(unsigned*)(p.ws + OFF_CNT + lane * 256) = 0u;
  asm volatile("s_waitcnt lgkmcnt(0)" ::: "memory");
}

}
extern "C" __global__ void __launch_bounds__(768, 3) gm_k1_kernel(Params1 p1) {
  extern __shared__ __attribute__((aligned(16))) unsigned char smem[];
  const Params& p = p1.p;
  STAMP_DECL;
  STAMP(0);
  if (threadIdx.x >= 640 && threadIdx.x < 704 && ((blockIdx.x >> 3) & 7) == 0) {
    unsigned lo1, hi1, lo2, hi2;
    asm volatile("s_getpc_b64 s[40:41]\n\ts_add_u32 s40, s40, gm_k1_kernel@rel32@lo+4\n\ts_addc_u32 s41, s41, gm_k1_kernel@rel32@hi+12\n\t"
                 "s_mov_b32 %0, s40\n\ts_mov_b32 %1, s41\n\t"
                 "s_getpc_b64 s[40:41]\n\ts_add_u32 s40, s40, gm_k2_kernel@rel32@lo+4\n\ts_addc_u32 s41, s41, gm_k2_kernel@rel32@hi+12\n\t"
                 "s_mov_b32 %2, s40\n\ts_mov_b32 %3, s41"
                 : "=s"(lo1), "=s"(hi1), "=s"(lo2), "=s"(hi2) :: "s40", "s41", "scc");
    const unsigned char* c1 = (const unsigned char*)(((unsigned long long)hi1 << 32) | lo1);
    const unsigned char* c2 = (const unsigned char*)(((unsigned long long)hi2 << 32) | lo2);
    const int ln = threadIdx.x & 63;
    unsigned acc = 0;
#pragma unroll
    for (int i = 0; i < CODE1_KB / 8; ++i) acc ^= *(const volatile unsigned*)(c1 + (i * 64 + ln) * 128);
#pragma unroll
    for (int i = 0; i < (CODE2_LINES + 63) / 64; ++i) {
      const int l = i * 64 + ln;
      acc ^= *(const volatile unsigned*)(c2 + (l < CODE2_LINES ? l : CODE2_LINES - 1) * 128);
    }
    asm volatile("" :: "v"(acc));
  }
  const unsigned t = p1.tm.v[blockIdx.x];
  if (t != 0xFFFFu) {
    const int type = (t >> 12) & 15, rt = (t >> 6) & 63, ct = t & 63;
    if (type == 0) k1_tile<0>(p, smem, rt, ct, t >> 16 STAMP_ARG);
    else if (type == 1) k1_tile<1>(p, smem, rt, ct, t >> 16 STAMP_ARG);
    else k1_tile<2>(p, smem, rt, ct, t >> 16 STAMP_ARG);
  } else {
    const int w = __builtin_amdgcn_readfirstlane(threadIdx.x >> 6);
    if (w >= 10) k1_side(p, smem, w - 10, nullptr);
  }
  STAMP(5);
  if (threadIdx.x < 256) STAMP_STORE(OFF_DBG1, 4);
}
namespace {

constexpr int AUX_SC1 = 16;
constexpr int RSRC_W3 = 0x00020000;
constexpr int K2_LDS = 4 * 25600;

DI unsigned kvf_pair(const float* slab, int head, int ls, int jp) {
  const int f = ls >> 6, l2 = ls & 63, rho = l2 & 15, g2 = l2 >> 4, hh = head & 3;
  int e;
  if (f == 0) e = 5 * (rho >> 2) + (rho & 3);
  else e = (rho & 3) == 0 ? 5 * (rho >> 2) + 4 : ((rho & 3) == 1 ? 20 : -1);
  float v[2];
#pragma unroll
  for (int q = 0; q < 2; ++q) {
    const int j = 2 * jp + q;
    const int d = 16 * (j >> 2) + 4 * g2 + (j & 3) - 4 * hh;
    const bool ok = (e >= 0) && (d >= 0) && (d < 20);
    const int off = ok ? d * 21 + e : 0;
    float s = 0.f;
#pragma unroll
    for (int pp = 0; pp < 11; ++pp) s += slab[(size_t)(pp * 32 + head) * 420 + off];
    v[q] = ok ? s : 0.f;
  }
  return pack2(v[0], v[1]);
}

}
extern "C" __global__ void __launch_bounds__(256, 1) gm_k2_kernel(Params p) {
  extern __shared__ __attribute__((aligned(16))) unsigned char smem[];
  const int tid = threadIdx.x, lane = tid & 63, w = __builtin_amdgcn_readfirstlane(tid >> 6);
  const int g = lane >> 4, li = lane & 15;
  const int b = blockIdx.x, bi = b >> 3;
  const int rb = 4 * (b & 7) + (bi >> 3), nb = bi & 7;
  const __amdgpu_buffer_rsrc_t rs = __builtin_amdgcn_make_buffer_rsrc((void*)p.ws, 0, (int)WS_BYTES, RSRC_W3);
  const float* slab = (const float*)(p.ws + OFF_SLAB);
  GAS unsigned* cnt = (GAS unsigned*)(p.ws + OFF_CNT);
  GAS unsigned* mycnt = cnt + (8 * w + (lane & 7)) * 64;
  STAMP_DECL;
  STAMP(0);
#define K2_DEPTH 3
  constexpr int NSTEP = 14, DEPTH = K2_DEPTH;
  const int T0 = 14 * w;
  const int cntw = w < 3 ? 14 : 12;
  const int vo = lane * 16;
  const int hbase = (int)OFF_HF + (4 * rb * 54) * 1024;
  const int wbase = (int)OFF_W2F + (nb * 54 * 5) * 1024;
  v4u hf[DEPTH][4], wf[DEPTH][5];
  const int wobase = (int)OFF_WOF + ((nb * 20 + 5 * w) * 5) * 1024;
  auto ld_step = [&](int i, v4u (&h)[4], v4u (&ww)[5]) {
    if (i < NSTEP) {
      const int T = T0 + (i < 12 ? i : (i < cntw ? i : cntw - 1));
#pragma unroll
      for (int m = 0; m < 4; ++m) h[m] = __builtin_amdgcn_raw_buffer_load_b128(rs, vo, hbase + (m * 54 + T) * 1024, 0);
#pragma unroll
      for (int t = 0; t < 5; ++t) ww[t] = __builtin_amdgcn_raw_buffer_load_b128(rs, vo, wbase + (T * 5 + t) * 1024, 0);
    } else {
#pragma unroll
      for (int t = 0; t < 5; ++t) ww[t] = __builtin_amdgcn_raw_buffer_load_b128(rs, vo, wobase + ((i - NSTEP) * 5 + t) * 1024, 0);
    }
  };
  if (w == 3) {
    const int head = b >> 3, ls = (b & 7) * 16 + (lane >> 2), jp = lane & 3;
    const unsigned v = kvf_pair(slab, head, ls, jp);
    __builtin_amdgcn_raw_buffer_store_b32(v, rs, (int)OFF_KVF + head * 2048 + ls * 16 + jp * 4, 0, AUX_SC1);
    asm volatile("s_waitcnt vmcnt(0)" ::: "memory");
    if (lane == 0) __hip_atomic_fetch_add(cnt + head * 64, 1u, __ATOMIC_RELAXED, __HIP_MEMORY_SCOPE_AGENT);
  }
  __builtin_amdgcn_sched_barrier(0);
#pragma unroll
  for (int i = 0; i < DEPTH; ++i) ld_step(i, hf[i], wf[i]);
  f32x2 gv[4];
#pragma unroll
  for (int m = 0; m < 4; ++m) gv[m] = *(const f32x2*)(p.ws + OFF_G + (size_t)(64 * rb + 16 * m + li) * 8);
  __builtin_amdgcn_sched_barrier(0);
  __builtin_amdgcn_sched_barrier(0);
  STAMP(1);
  f32x4 acc[5][4];
#pragma unroll
  for (int t = 0; t < 5; ++t)
#pragma unroll
    for (int m = 0; m < 4; ++m) acc[t][m] = f32x4{0.f, 0.f, 0.f, 0.f};
  v4u pa[4], pb[4], kvf[4][2], of[4][5];
  v2u pc[4];
  unsigned pollv = 0;
  int kvbase = (int)OFF_KVF + 8 * w * 2048;
  auto ld_half = [&](int gr) {
#pragma unroll
    for (int hh = 0; hh < 4; ++hh)
#pragma unroll
      for (int f = 0; f < 2; ++f)
        kvf[hh][f] = __builtin_amdgcn_raw_buffer_load_b128(rs, vo, kvbase + (4 * gr + hh) * 2048 + f * 1024, AUX_SC1);
#pragma unroll
    for (int m = 0; m < 4; ++m) {
      const int o = (int)OFF_PQ + ((4 * rb + m) * 8 + 2 * w + gr) * 2560;
      pa[m] = __builtin_amdgcn_raw_buffer_load_b128(rs, vo, o, 0);
      pb[m] = __builtin_amdgcn_raw_buffer_load_b128(rs, vo, o + 1024, 0);
      pc[m] = __builtin_amdgcn_raw_buffer_load_b64(rs, lane * 8, o + 2048, 0);
    }
  };
  auto o_calc = [&](int m, int gr) {
    float ov[20];
#pragma unroll
    for (int hh = 0; hh < 4; ++hh) {
      v4u bf;
      if (hh == 0) bf = pa[m];
      else if (hh == 1) bf = v4u{pa[m].z, pa[m].w, pb[m].x, pb[m].y};
      else if (hh == 2) bf = pb[m];
      else bf = v4u{pb[m].z, pb[m].w, pc[m].x, pc[m].y};
      const f32x4 z4 = f32x4{0.f, 0.f, 0.f, 0.f};
      const f32x4 c0 = mfma32(kvf[hh][0], bf, z4);
      const f32x4 c1 = mfma32(kvf[hh][1], bf, z4);
      const float sc = gv[m].x * __builtin_amdgcn_rcpf(c1[1] + 1e-6f);
      ov[5 * hh + 0] = c0[0] * sc;
      ov[5 * hh + 1] = c0[1] * sc;
      ov[5 * hh + 2] = c0[2] * sc;
      ov[5 * hh + 3] = c0[3] * sc;
      ov[5 * hh + 4] = c1[0] * sc;
    }
    if (gr == 0) {
      of[m][0] = v4u{pack2(ov[0], ov[1]), pack2(ov[2], ov[3]), pack2(ov[4], ov[5]), pack2(ov[6], ov[7])};
      of[m][1] = v4u{pack2(ov[8], ov[9]), pack2(ov[10], ov[11]), pack2(ov[12], ov[13]), pack2(ov[14], ov[15])};
      of[m][2].x = pack2(ov[16], ov[17]);
      of[m][2].y = pack2(ov[18], ov[19]);
    } else {
      of[m][2].z = pack2(ov[0], ov[1]);
      of[m][2].w = pack2(ov[2], ov[3]);
      of[m][3] = v4u{pack2(ov[4], ov[5]), pack2(ov[6], ov[7]), pack2(ov[8], ov[9]), pack2(ov[10], ov[11])};
      of[m][4] = v4u{pack2(ov[12], ov[13]), pack2(ov[14], ov[15]), pack2(ov[16], ov[17]), pack2(ov[18], ov[19])};
    }
  };
  static_for<NSTEP + 5>([&](auto IC) {
    constexpr int i = decltype(IC)::value;
    if (i == NSTEP - 1 || i == NSTEP - 2) {
      const unsigned msk = cntw == 14 ? 0xFFFFFFFFu : 0u;
#pragma unroll
      for (int m = 0; m < 4; ++m) hf[i % DEPTH][m] &= v4u{msk, msk, msk, msk};
    }
    if (i == NSTEP) {
#pragma unroll
      for (int t = 0; t < 5; ++t)
#pragma unroll
        for (int m = 0; m < 4; ++m) acc[t][m] *= gv[m].y;
      __builtin_amdgcn_sched_barrier(0);
    }
#pragma unroll
    for (int t = 0; t < 5; ++t)
#pragma unroll
      for (int m = 0; m < 4; ++m) {
        acc[t][m] = mfma32(wf[i % DEPTH][t], i < NSTEP ? hf[i % DEPTH][m] : of[m][i < NSTEP ? 0 : i - NSTEP], acc[t][m]);
      }
    if (i + DEPTH < NSTEP + 5) ld_step(i + DEPTH, hf[i % DEPTH], wf[i % DEPTH]);
    if (i == 8) pollv = __hip_atomic_load(mycnt, __ATOMIC_RELAXED, __HIP_MEMORY_SCOPE_AGENT);
    if (i == 10) {
      bool ok = __all(pollv == 8u);
      for (int spins = 0; !ok && spins < 200000; ++spins) {
        __builtin_amdgcn_s_sleep(16);
        const unsigned c = __hip_atomic_load(mycnt, __ATOMIC_RELAXED, __HIP_MEMORY_SCOPE_AGENT);
        ok = __all(c == 8u);
      }
      __builtin_amdgcn_fence(__ATOMIC_ACQUIRE, "wavefront");
      if (!ok) {
        const int fb = (int)OFF_FB + b * 65536 + 8 * w * 2048;
        for (int it = 0; it < 8 * 8; ++it) {
          const int hl = it >> 3, ls = (it & 7) * 16 + (lane >> 2), jp = lane & 3;
          const unsigned v = kvf_pair(slab, 8 * w + hl, ls, jp);
          __builtin_amdgcn_raw_buffer_store_b32(v, rs, fb + hl * 2048 + ls * 16 + jp * 4, 0, AUX_SC1);
        }
        asm volatile("s_waitcnt vmcnt(0)" ::: "memory");
        kvbase = fb;
      }
      ld_half(0);
    }
    if (i == 12) {
#pragma unroll
      for (int m = 0; m < 4; ++m) { __builtin_amdgcn_sched_barrier(0); o_calc(m, 0); __builtin_amdgcn_sched_barrier(0); }
      ld_half(1);
    }
    if (i == 14) {
#pragma unroll
      for (int m = 0; m < 4; ++m) { __builtin_amdgcn_sched_barrier(0); o_calc(m, 1); __builtin_amdgcn_sched_barrier(0); }
    }
    __builtin_amdgcn_sched_barrier(0);
  });
  __builtin_amdgcn_sched_barrier(0);
  f32x4* red = (f32x4*)smem;
#pragma unroll
  for (int t = 0; t < 5; ++t)
#pragma unroll
    for (int m = 0; m < 4; ++m) red[w * 1600 + (m * 5 + t) * 64 + lane] = acc[t][m];
  v2u bb[5];
#pragma unroll
  for (int t = 0; t < 5; ++t) bb[t] = *(const v2u*)(p.b2 + 80 * nb + 16 * t + 4 * g);
  __syncthreads();
  const float omg = w == 0 ? gv[0].y : (w == 1 ? gv[1].y : (w == 2 ? gv[2].y : gv[3].y));
  u16* orow = p.out + (size_t)(64 * rb + 16 * w + li) * D_ + 80 * nb + 4 * g;
#pragma unroll
  for (int t = 0; t < 5; ++t) {
    f32x4 s = red[(w * 5 + t) * 64 + lane];
#pragma unroll
    for (int ww = 1; ww < 4; ++ww) s += red[ww * 1600 + (w * 5 + t) * 64 + lane];
    const float o0 = s[0] + omg * bf2f(bb[t].x & 0xFFFFu), o1 = s[1] + omg * bf2f(bb[t].x >> 16);
    const float o2 = s[2] + omg * bf2f(bb[t].y & 0xFFFFu), o3 = s[3] + omg * bf2f(bb[t].y >> 16);
    *(v2u*)(orow + 16 * t) = v2u{pack2(o0, o1), pack2(o2, o3)};
  }
  STAMP(6);
  STAMP_STORE(OFF_DBG2, 4);
}

extern "C" void kernel_launch(void* const* d_in, const int* in_sizes, int n_in, void* d_out, int out_size, void* d_ws,
                              size_t ws_size, hipStream_t stream) {
  Params p;
  p.x = (const u16*)d_in[0];
  p.wqkv = (const u16*)d_in[1];
  p.wo = (const u16*)d_in[2];
  p.w1 = (const u16*)d_in[4];
  p.b1 = (const u16*)d_in[5];
  p.w2 = (const u16*)d_in[6];
  p.b2 = (const u16*)d_in[7];
  p.gw = (const u16*)d_in[8];
  p.out = (u16*)d_out;
  p.ws = (unsigned char*)d_ws;
  (void)hipFuncSetAttribute((const void*)gm_k1_kernel, hipFuncAttributeMaxDynamicSharedMemorySize, K1_LDS);
  (void)hipFuncSetAttribute((const void*)gm_k2_kernel, hipFuncAttributeMaxDynamicSharedMemorySize, K2_LDS);
  Params1 p1;
  p1.p = p;
  constexpr TileMap tm = make_tilemap();
  p1.tm = tm;
  gm_k1_kernel<<<256, 768, K1_LDS, stream>>>(p1);
  gm_k2_kernel<<<256, 256, K2_LDS, stream>>>(p);
}
```

```cpp
#include <hip/hip_runtime.h>
#include <hip/hip_bf16.h>
#include <stdint.h>
#include <type_traits>
#include <utility>

namespace {

constexpr int S_ = 2048, D_ = 640, F_ = 1728;
typedef unsigned short u16;
typedef unsigned v4u __attribute__((ext_vector_type(4)));
typedef unsigned v2u __attribute__((ext_vector_type(2)));
typedef float f32x4 __attribute__((ext_vector_type(4)));
typedef float f32x2 __attribute__((ext_vector_type(2)));
typedef short bf16x8 __attribute__((ext_vector_type(8)));
typedef short bf16x4 __attribute__((ext_vector_type(4)));
typedef short v4i16_t __attribute__((ext_vector_type(4)));
typedef __bf16 bf2_t __attribute__((ext_vector_type(2)));
#define LAS __attribute__((address_space(3)))
#define GAS __attribute__((address_space(1)))
#define DI __device__ __forceinline__

constexpr size_t OFF_CNT = 0;
constexpr size_t OFF_G = 32 * 256;
constexpr size_t OFF_KVF = OFF_G + 2048 * 8;
constexpr size_t OFF_SLAB = OFF_KVF + 65536;
constexpr size_t SLAB_BYTES = ((size_t)11 * 32 * 420 * 4 + 255) / 256 * 256;
constexpr size_t OFF_W2F = OFF_SLAB + SLAB_BYTES;
constexpr size_t OFF_WOF = OFF_W2F + (size_t)8 * 54 * 5 * 1024;
constexpr size_t OFF_PQ = OFF_WOF + (size_t)8 * 20 * 5 * 1024;
constexpr size_t OFF_HF = OFF_PQ + (size_t)128 * 8 * 2560;
constexpr size_t OFF_FB = OFF_HF + (size_t)128 * 54 * 1024;
constexpr size_t OFF_DUMP = OFF_FB + (size_t)256 * 65536;
constexpr size_t WS_BYTES = OFF_DUMP + 65536;
static_assert(OFF_SLAB % 256 == 0 && OFF_W2F % 256 == 0 && OFF_PQ % 256 == 0 && OFF_HF % 256 == 0, "align");
static_assert(WS_BYTES < (1ull << 31), "int offsets");

struct Params {
  const u16* x;
  const u16* wqkv;
  const u16* wo;
  const u16* w1;
  const u16* b1;
  const u16* w2;
  const u16* b2;
  const u16* gw;
  u16* out;
  unsigned char* ws;
};

template <int... I, class Fn> DI void static_for_impl(std::integer_sequence<int, I...>, Fn&& fn) { (fn(std::integral_constant<int, I>{}), ...); }
template <int N, class Fn> DI void static_for(Fn&& fn) { static_for_impl(std::make_integer_sequence<int, N>{}, fn); }
DI float bf2f(unsigned v) { return __builtin_bit_cast(float, v << 16); }
DI float rbf(float a) { return (float)(__bf16)a; }
DI unsigned pack2(float a, float b) {
  f32x2 v = {a, b};
  bf2_t r = __builtin_convertvector(v, bf2_t);
  return __builtin_bit_cast(unsigned, r);
}
DI float phi_f(float t) {
  const float e = __builtin_amdgcn_exp2f(t * 1.4426950408889634f);
  return (t > 0.f ? t + 1.f : e) + 1e-6f;
}
DI f32x2 gelu2(f32x2 x) {
  const f32x2 c1 = {-2.302208198f, -2.302208198f}, c2 = {-0.1029432397f, -0.1029432397f};
  const f32x2 e = x * (x * x * c2 + c1);
  const f32x2 d = f32x2{__builtin_amdgcn_exp2f(e.x), __builtin_amdgcn_exp2f(e.y)} + f32x2{1.f, 1.f};
  return x * f32x2{__builtin_amdgcn_rcpf(d.x), __builtin_amdgcn_rcpf(d.y)};
}
DI f32x4 mfma32(v4u a, v4u b, f32x4 c) {
  return __builtin_amdgcn_mfma_f32_16x16x32_bf16(__builtin_bit_cast(bf16x8, a), __builtin_bit_cast(bf16x8, b), c, 0, 0, 0);
}
DI f32x4 mfma16(v2u a, v2u b, f32x4 c) {
  return __builtin_amdgcn_mfma_f32_16x16x16bf16_1k(__builtin_bit_cast(bf16x4, a), __builtin_bit_cast(bf16x4, b), c, 0, 0, 0);
}
DI v2u tr_read(const unsigned char* p) {
  return __builtin_bit_cast(v2u, __builtin_amdgcn_ds_read_tr16_b64_v4i16((LAS v4i16_t*)p));
}
DI void glds16(unsigned voff, const void* sbase, unsigned lds_dst) {
  unsigned keep;
  asm volatile("s_mov_b32 %0, m0\n\ts_mov_b32 m0, %3\n\ts_nop 0\n\tglobal_load_lds_dwordx4 %1, %2\n\ts_mov_b32 m0, %0"
               : "=&s"(keep) : "v"(voff), "s"(sbase), "s"(lds_dst) : "memory");
}
#define STAMP(i)
#define STAMP_DECL
#define STAMP_PARAM
#define STAMP_ARG
#define STAMP_STORE(base, nw)
constexpr size_t OFF_DBG1 = (size_t)64 << 20, OFF_DBG2 = (size_t)65 << 20;

struct TileMap { unsigned v[256]; };
constexpr TileMap make_tilemap() {
  TileMap m{};
  for (int i = 0; i < 256; ++i) m.v[i] = 0xFFFF;
  for (int half = 0; half < 2; ++half) {
    const int q0 = half ? 6 : 0, q1 = half ? 11 : 6, h0 = half ? 6 : 0, h1 = half ? 13 : 6;
    for (int xq = 0; xq < 4; ++xq) {
      int n = 0;
      const int x = half * 4 + xq;
      for (int c = 0; c < 2; ++c)
        for (int rt = q0; rt < q1; ++rt) m.v[x + 8 * n++] = (unsigned)((1 << 12) | (rt << 6) | (2 * xq + c));
      for (int rt = q0; rt < q1; ++rt) m.v[x + 8 * n++] = (unsigned)((0 << 12) | (rt << 6) | xq);
      for (int c = 0; c < 2; ++c)
        for (int rt = h0; rt < h1; ++rt) m.v[x + 8 * n++] = (unsigned)((2 << 12) | (rt << 6) | (2 * xq + c));
      for (int rt = h0; rt < h1; ++rt)
        if (((rt - h0) & 3) == xq) m.v[x + 8 * n++] = (unsigned)((2 << 12) | (rt << 6) | 8);
    }
  }
  int nidle = 0, nqh = 0;
  for (int b = 0; b < 256; ++b) {
    const unsigned t = m.v[b] & 0xFFFF;
    if (t == 0xFFFF) m.v[b] = t | (unsigned)(nidle++) << 16;
    else if ((t >> 12) != 1) m.v[b] = t | (unsigned)(nqh++) << 16;
  }
  return m;
}
struct Params1 {
  Params p;
  TileMap tm;
};

constexpr int K1_STAGE = 45056;
constexpr int K1_LDS = 3 * K1_STAGE + 512 + 16384;

DI void k1_side(const Params& p, unsigned char* smem, int w, const u16* bias, int unit);

template <int TYPE>
DI void k1_tile(const Params& p, unsigned char* smem, int rt, int ct, unsigned share STAMP_PARAM) {
  constexpr int MT = TYPE == 2 ? 5 : 3, NTW = TYPE == 2 ? 3 : 5, WM = TYPE == 2 ? 2 : 4, WN = TYPE == 2 ? 4 : 2;
  constexpr int BM = MT * 16 * WM, BN = NTW * 16 * WN, NT = BN / 16, XB = BM * 128, NMM = MT * NTW;
  constexpr int NX = BM / 32, NW = NT / 2;
  constexpr int NRD = MT + 2 * NTW;
  static_assert(XB + 64 * BN * 2 == K1_STAGE && NX + NW == 11, "stage");
  const int tid = threadIdx.x, lane = tid & 63, w = __builtin_amdgcn_readfirstlane(tid >> 6);
  const int wm = w / WN, wn = w % WN, g = lane >> 4, li = lane & 15;
  if (w >= 10) {
    k1_side(p, smem, w - 10, TYPE == 2 ? p.b1 + 192 * ct : nullptr, TYPE == 1 ? -1 : (int)share * 2 + (w - 10));
    return;
  }
  const int rv = rt * BM, r0 = rv + BM <= S_ ? rv : S_ - BM;
  const u16* W = TYPE == 2 ? p.w1 : p.wqkv;
  constexpr int LDW = TYPE == 2 ? F_ : 3 * D_;
  const unsigned srcx = (unsigned)((r0 + (lane >> 3)) * D_ + 8 * ((lane & 7) ^ (lane >> 3))) * 2u;
  unsigned src[NW];
#pragma unroll
  for (int i = 0; i < NW; ++i) {
    const int cc = 64 * i + lane;
    const int kbl = cc / (16 * NT), c2 = cc % (16 * NT);
    const int nt = c2 >> 4, rowp = (c2 >> 1) & 7, half = c2 & 1;
    const int k = 8 * kbl + (rowp ^ (4 * kbl));
    const int col = 16 * nt + 8 * half;
    int gcol;
    if (TYPE == 0) gcol = 160 * ct + col;
    else if (TYPE == 1) {
      gcol = (col < 80 ? D_ + col : 2 * D_ + col - 80) + 80 * ct;
    } else gcol = 192 * ct + col;
    src[i] = (unsigned)(k * LDW + gcol) * 2u;
  }
  const unsigned lds0 = (unsigned)(uintptr_t)(LAS unsigned char*)smem;
  constexpr int WB = K1_STAGE - XB;
  auto issue1 = [&](int i, int wv, int kt, int st) {
    if (i < NX) glds16(srcx, p.x + kt * 64 + (wv * (BM / 4) + i * 8) * D_, lds0 + st * XB + (wv * BM * 2 + 64 * i) * 16);
    else glds16(src[i - NX], W + (size_t)(kt * 64 + wv * 16) * LDW, lds0 + 3 * XB + st * WB + (wv * 32 * NT + 64 * (i - NX)) * 16);
  };
  if (w >= 8) {
    const int q0 = 2 * (w - 8);
#pragma unroll
    for (int t = 1; t < 3; ++t)
#pragma unroll
      for (int i = 0; i < 22; ++i) issue1(i % 11, q0 + i / 11, t, t);
    __builtin_amdgcn_s_barrier();
    int bf = 0;
#pragma unroll 1
    for (int kt = 0; kt < 9; ++kt) {
      if (kt < 8) asm volatile("s_waitcnt vmcnt(22)" ::: "memory");
      else asm volatile("s_waitcnt vmcnt(0)" ::: "memory");
      __builtin_amdgcn_s_barrier();
      if (kt + 3 < 10) {
#pragma unroll
        for (int i = 0; i < 22; ++i) issue1(i % 11, q0 + i / 11, kt + 3, bf);
      }
      bf = bf == 2 ? 0 : bf + 1;
    }
    return;
  }
#pragma unroll
  for (int i = 0; i < 11; ++i)
    if ((i & 1) == (w & 1)) issue1(i, w >> 1, 0, 0);
  STAMP(1);
  const unsigned xoff0 = (unsigned)((wm * MT * 16 + li) * 128 + ((g ^ (lane & 7)) << 4));
  const unsigned woff0 = (unsigned)(3 * XB + g * NT * 256 + wn * NTW * 256 + (4 * (g & 1) + (li >> 2)) * 32 + (lane & 3) * 8);
  unsigned woffk[5];
#pragma unroll
  for (int b = 0; b < 5; ++b) {
    const int cc = 16 * b + 4 * (lane & 3);
    const int col = cc < 40 ? 40 * wn + cc : 40 + 40 * wn + cc;
    woffk[b] = (unsigned)(3 * XB + g * NT * 256 + (col >> 4) * 256 + (4 * (g & 1) + (li >> 2)) * 32 + (col & 15) * 2);
  }
  auto raw_tile = [&](int ww, int i) {
    return (f32x4*)(smem + (ww < 4 ? XB + ww * 10240 : 3 * XB + WB + (ww - 4) * 10240) + i * 1024);
  };
  unsigned goff[2] = {0u, 0u};
  if (TYPE == 1) {
#pragma unroll
    for (int q = 0; q < 4; ++q) {
      const int o = (4 * tid + q) % 840, hh = o / 420, rem = o % 420, dd = rem / 21, e = rem % 21;
      const int c1 = 20 * hh + dd, n1 = c1 >> 4, row = c1 & 15;
      const int c2 = 40 + 20 * hh + e, n2 = c2 >> 4;
      const int tile = e < 20 ? (n1 == 0 ? n2 - 2 : (n1 == 1 ? n2 : n2 + 2)) : 7 + n1;
      const int col = e < 20 ? (c2 & 15) : 0;
      goff[q >> 1] |= (unsigned)(tile * 256 + ((row >> 2) * 16 + col) * 4 + (row & 3)) << (16 * (q & 1));
    }
    asm volatile("" : "+v"(goff[0]), "+v"(goff[1]));
  }
  unsigned char* wsb = p.ws;
  asm volatile("" : "+s"(wsb));
  f32x4 acc[MT][NTW];
#pragma unroll
  for (int a = 0; a < MT; ++a)
#pragma unroll
    for (int b = 0; b < NTW; ++b) acc[a][b] = f32x4{0.f, 0.f, 0.f, 0.f};
  v4u xa[MT], xb[MT];
  v2u wa[NTW][2], wb[NTW][2];
  auto rd1 = [&](int r, int ks, int st, v4u (&xf)[MT], v2u (&wf)[NTW][2]) {
    if (r < MT) xf[r] = *(const v4u*)(smem + (xoff0 ^ (ks * 64)) + (st * XB + r * 2048));
    else {
      const int b = (r - MT) >> 1, hi = (r - MT) & 1;
      if (TYPE == 1) wf[b][hi] = tr_read(smem + (woffk[b < 5 ? b : 0] ^ (hi * 128)) + (st * WB + ks * 4 * NT * 256));
      else wf[b][hi] = tr_read(smem + (woff0 ^ (hi * 128)) + (st * WB + ks * 4 * NT * 256 + b * 256));
    }
  };
  auto mm = [&](int a, int b, v4u (&xf)[MT], v2u (&wf)[NTW][2]) {
    const v4u wv = v4u{wf[b][0].x, wf[b][0].y, wf[b][1].x, wf[b][1].y};
    acc[a][b] = TYPE == 1 ? mfma32(xf[a], wv, acc[a][b]) : mfma32(wv, xf[a], acc[a][b]);
  };
  asm volatile("s_waitcnt vmcnt(0)" ::: "memory");
  __builtin_amdgcn_s_barrier();
  STAMP(2);
#pragma unroll
  for (int r = 0; r < NRD; ++r) rd1(r, 0, 0, xa, wa);
  auto body = [&](int kt, auto MODE, auto BUF) {
    constexpr int mode = decltype(MODE)::value, sb = decltype(BUF)::value, sn = (sb + 1) % 3;
    __builtin_amdgcn_sched_barrier(0);
#pragma unroll
    for (int i = 0; i < NMM; ++i) {
      mm(i / NTW, i % NTW, xa, wa);
      if (i < NRD) rd1(i, 1, sb, xb, wb);
      __builtin_amdgcn_sched_barrier(0);
    }
    if (mode != 3) {
      asm volatile("s_waitcnt lgkmcnt(0)" ::: "memory");
      __builtin_amdgcn_s_barrier();
    }
    __builtin_amdgcn_sched_barrier(0);
#pragma unroll
    for (int i = 0; i < NMM; ++i) {
      mm(i / NTW, i % NTW, xb, wb);
      if (mode != 3 && i < NRD) rd1(i, 0, sn, xa, wa);
      __builtin_amdgcn_sched_barrier(0);
    }
  };
#pragma unroll 1
  for (int kt = 0; kt < 9; kt += 3) {
    body(kt, std::integral_constant<int, 0>{}, std::integral_constant<int, 0>{});
    body(kt + 1, std::integral_constant<int, 0>{}, std::integral_constant<int, 1>{});
    body(kt + 2, std::integral_constant<int, 0>{}, std::integral_constant<int, 2>{});
  }
  body(9, std::integral_constant<int, 3>{}, std::integral_constant<int, 0>{});
  __builtin_amdgcn_sched_barrier(0);
  STAMP(4);
  __builtin_amdgcn_sched_barrier(0);
  if (TYPE == 0) {
    const int gq = 2 * ct + wn;
#pragma unroll
    for (int a = 0; a < MT; ++a) {
      const int st = (r0 + wm * 48 + 16 * a) >> 4;
      {
        unsigned pk[5][2];
#pragma unroll
        for (int b = 0; b < 5; ++b) {
          pk[b][0] = pack2(phi_f(acc[a][b][0]), phi_f(acc[a][b][1]));
          pk[b][1] = pack2(phi_f(acc[a][b][2]), phi_f(acc[a][b][3]));
        }
        unsigned char* dst = wsb + (st * 16 >= rv ? (unsigned)OFF_PQ + (unsigned)(st * 8 + gq) * 2560u : (unsigned)OFF_DUMP);
        *(v4u*)(dst + lane * 16) = v4u{pk[0][0], pk[0][1], pk[1][0], pk[1][1]};
        *(v4u*)(dst + 1024 + lane * 16) = v4u{pk[2][0], pk[2][1], pk[3][0], pk[3][1]};
        *(v2u*)(dst + 2048 + lane * 8) = v2u{pk[4][0], pk[4][1]};
      }
    }
  } else if (TYPE == 2) {
    f32x2 bias[3][2];
#pragma unroll
    for (int b = 0; b < 3; ++b) {
      const v2u bb = *(const v2u*)(smem + 3 * K1_STAGE + (48 * wn + 16 * b + 4 * g) * 2);
      bias[b][0] = f32x2{bf2f(bb.x & 0xFFFFu), bf2f(bb.x >> 16)};
      bias[b][1] = f32x2{bf2f(bb.y & 0xFFFFu), bf2f(bb.y >> 16)};
    }
    const int ntg = 12 * ct + 3 * wn;
#pragma unroll
    for (int a = 0; a < MT; ++a) {
      const int st = (r0 + wm * 80 + 16 * a) >> 4;
      unsigned pk[3][2];
#pragma unroll
      for (int b = 0; b < 3; ++b)
#pragma unroll
        for (int hf = 0; hf < 2; ++hf) {
          const f32x2 y = gelu2(f32x2{acc[a][b][2 * hf], acc[a][b][2 * hf + 1]} + bias[b][hf]);
          pk[b][hf] = pack2(y.x, y.y);
        }
      unsigned char* dst = wsb + (st * 16 >= rv ? (unsigned)OFF_HF + (unsigned)(st * 54 + (ntg >> 1)) * 1024u : (unsigned)OFF_DUMP) + lane * 16;
      if ((wn & 1) == 0) {
        *(v4u*)dst = v4u{pk[0][0], pk[0][1], pk[1][0], pk[1][1]};
        *(v2u*)(dst + 1024) = v2u{pk[2][0], pk[2][1]};
      } else {
        *(v2u*)(dst + 8) = v2u{pk[0][0], pk[0][1]};
        *(v4u*)(dst + 1024) = v4u{pk[1][0], pk[1][1], pk[2][0], pk[2][1]};
      }
    }
  } else {
    const v2u ones = v2u{0x3F803F80u, 0x3F803F80u};
    v2u fr[3][5];
#pragma unroll
    for (int a = 0; a < 3; ++a) {
      const bool valid = (r0 + wm * 48 + 16 * a) >= rv;
#pragma unroll
      for (int b = 0; b < 5; ++b) {
        const bool isk = (16 * b + li) < 40;
        float v[4];
#pragma unroll
        for (int r = 0; r < 4; ++r) {
          float t = acc[a][b][r];
          if (b < 2) t = phi_f(t);
          else if (b == 2) t = isk ? phi_f(t) : t;
          v[r] = valid ? t : 0.f;
        }
        fr[a][b] = v2u{pack2(v[0], v[1]), pack2(v[2], v[3])};
      }
    }
    __builtin_amdgcn_sched_barrier(0);
    f32x4 d[10];
#pragma unroll
    for (int i = 0; i < 10; ++i) d[i] = f32x4{0.f, 0.f, 0.f, 0.f};
#pragma unroll
    for (int a = 0; a < 3; ++a) {
      d[0] = mfma16(fr[a][0], fr[a][2], d[0]);
      d[1] = mfma16(fr[a][0], fr[a][3], d[1]);
      d[2] = mfma16(fr[a][1], fr[a][2], d[2]);
      d[3] = mfma16(fr[a][1], fr[a][3], d[3]);
      d[4] = mfma16(fr[a][1], fr[a][4], d[4]);
      d[5] = mfma16(fr[a][2], fr[a][3], d[5]);
      d[6] = mfma16(fr[a][2], fr[a][4], d[6]);
      d[7] = mfma16(fr[a][0], ones, d[7]);
      d[8] = mfma16(fr[a][1], ones, d[8]);
      d[9] = mfma16(fr[a][2], ones, d[9]);
    }
    __builtin_amdgcn_sched_barrier(0);
    f32x4* raw = (f32x4*)(smem + XB);
#pragma unroll
    for (int i = 0; i < 10; ++i) raw_tile(w, i)[lane] = d[i];
    __syncthreads();
    if (tid < 420) {
      const int cwn = tid / 210;
      f32x4 sum;
#pragma unroll
      for (int q = 0; q < 4; ++q) {
        const unsigned off = (goff[q >> 1] >> (16 * (q & 1))) & 0xFFFFu;
        float a = 0.f;
#pragma unroll
        for (int m = 0; m < 4; ++m) a += ((const float*)raw_tile(2 * m + cwn, 0))[off];
        sum[q] = a;
      }
      ((f32x4*)(wsb + OFF_SLAB))[(size_t)(rt * 32 + 4 * ct) * 105 + tid] = sum;
    }
    (void)raw;
  }
}

DI void retile_load(const Params& p, int id, unsigned scr_l, int lane) {
  id = id < 740 ? id : 739;
  const int Tt = id / 10, c4 = id % 10;
#pragma unroll
  for (int i = 0; i < 4; ++i) {
    const int j = lane >> 3;
    int row;
    if (Tt < 54) row = 32 * Tt + 16 * (j >> 2) + 4 * i + (j & 3);
    else {
      const int U = Tt - 54, ww = U / 5, u = U % 5, vv = 8 * u + j;
      row = (8 * ww + vv / 5) * 20 + 5 * i + vv % 5;
    }
    glds16((unsigned)(row * D_ + 64 * c4 + 8 * (lane & 7)) * 2u, Tt < 54 ? p.w2 : p.wo, scr_l + i * 1024);
  }
}
DI void retile_store(const Params& p, int id, const unsigned char* scr, int lane) {
  const int Tt = id / 10, c4 = id % 10, g = lane >> 4, li = lane & 15;
#pragma unroll
  for (int t4 = 0; t4 < 4; ++t4) {
    const unsigned char* a = scr + (8 * g + (li >> 2)) * 128 + t4 * 32 + (lane & 3) * 8;
    const v2u lo = tr_read(a), hi = tr_read(a + 512);
    const int ntg = 4 * c4 + t4, nb = ntg / 5, t = ntg % 5;
    const size_t o = Tt < 54 ? OFF_W2F + (size_t)((nb * 54 + Tt) * 5 + t) * 1024 : OFF_WOF + (size_t)((nb * 20 + Tt - 54) * 5 + t) * 1024;
    *(v4u*)(p.ws + (id < 740 ? o : OFF_DUMP) + lane * 16) = v4u{lo.x, lo.y, hi.x, hi.y};
  }
}

DI void k1_side(const Params& p, unsigned char* smem, int w, const u16* bias, int unit) {
  const int tid = threadIdx.x, lane = tid & 63;
  const int gw = blockIdx.x * 2 + w, g = lane >> 4, li = lane & 15;
  unsigned char* scr = smem + 3 * K1_STAGE + 512 + w * 4096;
  if (unit >= 0) retile_load(p, unit, (unsigned)(uintptr_t)(LAS unsigned char*)scr, lane);
  v4u xv[5], gv[5];
#pragma unroll
  for (int i = 0; i < 5; ++i) {
    gv[i] = *(const v4u*)(p.gw + (i * 16 + li) * 8);
    xv[i] = *(const v4u*)(p.x + (size_t)(4 * gw + g) * D_ + (i * 16 + li) * 8);
  }
  unsigned bias2 = 0u;
  if (bias) bias2 = ((const unsigned*)bias)[w * 48 + (lane < 48 ? lane : 47)];
  asm volatile("s_waitcnt vmcnt(0)" ::: "memory");
  __builtin_amdgcn_sched_barrier(0);
  if (bias && lane < 48) ((unsigned*)(smem + 3 * K1_STAGE))[w * 48 + lane] = bias2;
  if (unit >= 0) retile_store(p, unit, scr, lane);
  float s = 0.f;
#pragma unroll
  for (int i = 0; i < 5; ++i)
#pragma unroll
    for (int q = 0; q < 4; ++q) {
      s = __builtin_fmaf(bf2f(xv[i][q] & 0xFFFFu), bf2f(gv[i][q] & 0xFFFFu), s);
      s = __builtin_fmaf(__builtin_bit_cast(float, xv[i][q] & 0xFFFF0000u), __builtin_bit_cast(float, gv[i][q] & 0xFFFF0000u), s);
    }
#pragma unroll
  for (int o = 8; o > 0; o >>= 1) s += __shfl_xor(s, o);
  if (li == 0) {
    const float lg = rbf(s);
    const float gg = rbf(1.f / (1.f + __expf(-lg)));
    *(f32x2*)(p.ws + OFF_G + (size_t)(4 * gw + g) * 8) = f32x2{gg, rbf(1.f - gg)};
  }
  if (blockIdx.x == 0 && w == 0 && lane < 32) *(unsigned*)(p.ws + OFF_CNT + lane * 256) = 0u;
  asm volatile("s_waitcnt lgkmcnt(0)" ::: "memory");
}

DI void k1_retile(const Params& p, unsigned char* smem, int rank, int w) {
  const int lane = threadIdx.x & 63, gw = rank * 10 + w;
  unsigned char* scr = smem + w * 12288;
  const unsigned scr_l = (unsigned)(uintptr_t)(LAS unsigned char*)scr;
#pragma unroll 1
  for (int round = 0; round < 2; ++round) {
#pragma unroll
    for (int it = 0; it < 3; ++it) retile_load(p, 322 + gw + 70 * (3 * round + it), scr_l + it * 4096, lane);
    asm volatile("s_waitcnt vmcnt(0)" ::: "memory");
    __builtin_amdgcn_sched_barrier(0);
#pragma unroll
    for (int it = 0; it < 3; ++it) retile_store(p, 322 + gw + 70 * (3 * round + it), scr + it * 4096, lane);
    asm volatile("s_waitcnt lgkmcnt(0)" ::: "memory");
    __builtin_amdgcn_sched_barrier(0);
  }
}

}
extern "C" __global__ void __launch_bounds__(768, 3) gm_k1_kernel(Params1 p1) {
  extern __shared__ __attribute__((aligned(16))) unsigned char smem[];
  const Params& p = p1.p;
  STAMP_DECL;
  STAMP(0);
  const unsigned t = p1.tm.v[blockIdx.x];
  if ((t & 0xFFFFu) != 0xFFFFu) {
    const int type = (t >> 12) & 15, rt = (t >> 6) & 63, ct = t & 63;
    if (type == 0) k1_tile<0>(p, smem, rt, ct, t >> 16 STAMP_ARG);
    else if (type == 1) k1_tile<1>(p, smem, rt, ct, t >> 16 STAMP_ARG);
    else k1_tile<2>(p, smem, rt, ct, t >> 16 STAMP_ARG);
  } else {
    const int w = __builtin_amdgcn_readfirstlane(threadIdx.x >> 6);
    if (w >= 10) k1_side(p, smem, w - 10, nullptr, -1);
    else k1_retile(p, smem, (int)(t >> 16), w);
  }
  STAMP(5);
  if (threadIdx.x < 256) STAMP_STORE(OFF_DBG1, 4);
}
namespace {

constexpr int AUX_SC1 = 16;
constexpr int RSRC_W3 = 0x00020000;
constexpr int K2_LDS = 4 * 25600;

DI unsigned kvf_pair(const float* slab, int head, int ls, int jp) {
  const int f = ls >> 6, l2 = ls & 63, rho = l2 & 15, g2 = l2 >> 4, hh = head & 3;
  int e;
  if (f == 0) e = 5 * (rho >> 2) + (rho & 3);
  else e = (rho & 3) == 0 ? 5 * (rho >> 2) + 4 : ((rho & 3) == 1 ? 20 : -1);
  float v[2];
#pragma unroll
  for (int q = 0; q < 2; ++q) {
    const int j = 2 * jp + q;
    const int d = 16 * (j >> 2) + 4 * g2 + (j & 3) - 4 * hh;
    const bool ok = (e >= 0) && (d >= 0) && (d < 20);
    const int off = ok ? d * 21 + e : 0;
    float s = 0.f;
#pragma unroll
    for (int pp = 0; pp < 11; ++pp) s += slab[(size_t)(pp * 32 + head) * 420 + off];
    v[q] = ok ? s : 0.f;
  }
  return pack2(v[0], v[1]);
}

}
extern "C" __global__ void __launch_bounds__(256, 1) gm_k2_kernel(Params p) {
  extern __shared__ __attribute__((aligned(16))) unsigned char smem[];
  const int tid = threadIdx.x, lane = tid & 63, w = __builtin_amdgcn_readfirstlane(tid >> 6);
  const int g = lane >> 4, li = lane & 15;
  const int b = blockIdx.x, bi = b >> 3;
  const int rb = 4 * (b & 7) + (bi >> 3), nb = bi & 7;
  const __amdgpu_buffer_rsrc_t rs = __builtin_amdgcn_make_buffer_rsrc((void*)p.ws, 0, (int)WS_BYTES, RSRC_W3);
  const float* slab = (const float*)(p.ws + OFF_SLAB);
  GAS unsigned* cnt = (GAS unsigned*)(p.ws + OFF_CNT);
  GAS unsigned* mycnt = cnt + (8 * w + (lane & 7)) * 64;
  STAMP_DECL;
  STAMP(0);
#define K2_DEPTH 3
  constexpr int NSTEP = 14, DEPTH = K2_DEPTH;
  const int T0 = 14 * w;
  const int cntw = w < 3 ? 14 : 12;
  const int vo = lane * 16;
  const int hbase = (int)OFF_HF + (4 * rb * 54) * 1024;
  const int wbase = (int)OFF_W2F + (nb * 54 * 5) * 1024;
  v4u hf[DEPTH][4], wf[DEPTH][5];
  const int wobase = (int)OFF_WOF + ((nb * 20 + 5 * w) * 5) * 1024;
  auto ld_step = [&](int i, v4u (&h)[4], v4u (&ww)[5]) {
    if (i < NSTEP) {
      const int T = T0 + (i < 12 ? i : (i < cntw ? i : cntw - 1));
#pragma unroll
      for (int m = 0; m < 4; ++m) h[m] = __builtin_amdgcn_raw_buffer_load_b128(rs, vo, hbase + (m * 54 + T) * 1024, 0);
#pragma unroll
      for (int t = 0; t < 5; ++t) ww[t] = __builtin_amdgcn_raw_buffer_load_b128(rs, vo, wbase + (T * 5 + t) * 1024, 0);
    } else {
#pragma unroll
      for (int t = 0; t < 5; ++t) ww[t] = __builtin_amdgcn_raw_buffer_load_b128(rs, vo, wobase + ((i - NSTEP) * 5 + t) * 1024, 0);
    }
  };
  if (w == 3) {
    const int head = b >> 3, ls = (b & 7) * 16 + (lane >> 2), jp = lane & 3;
    const unsigned v = kvf_pair(slab, head, ls, jp);
    __builtin_amdgcn_raw_buffer_store_b32(v, rs, (int)OFF_KVF + head * 2048 + ls * 16 + jp * 4, 0, AUX_SC1);
    asm volatile("s_waitcnt vmcnt(0)" ::: "memory");
    if (lane == 0) __hip_atomic_fetch_add(cnt + head * 64, 1u, __ATOMIC_RELAXED, __HIP_MEMORY_SCOPE_AGENT);
  }
  __builtin_amdgcn_sched_barrier(0);
#pragma unroll
  for (int i = 0; i < DEPTH; ++i) ld_step(i, hf[i], wf[i]);
  f32x2 gv[4];
#pragma unroll
  for (int m = 0; m < 4; ++m) gv[m] = *(const f32x2*)(p.ws + OFF_G + (size_t)(64 * rb + 16 * m + li) * 8);
  __builtin_amdgcn_sched_barrier(0);
  __builtin_amdgcn_sched_barrier(0);
  STAMP(1);
  f32x4 acc[5][4];
#pragma unroll
  for (int t = 0; t < 5; ++t)
#pragma unroll
    for (int m = 0; m < 4; ++m) acc[t][m] = f32x4{0.f, 0.f, 0.f, 0.f};
  v4u pa[4], pb[4], kvf[4][2], of[4][5];
  v2u pc[4];
  unsigned pollv = 0;
  int kvbase = (int)OFF_KVF + 8 * w * 2048;
  auto ld_half = [&](int gr) {
#pragma unroll
    for (int hh = 0; hh < 4; ++hh)
#pragma unroll
      for (int f = 0; f < 2; ++f)
        kvf[hh][f] = __builtin_amdgcn_raw_buffer_load_b128(rs, vo, kvbase + (4 * gr + hh) * 2048 + f * 1024, AUX_SC1);
#pragma unroll
    for (int m = 0; m < 4; ++m) {
      const int o = (int)OFF_PQ + ((4 * rb + m) * 8 + 2 * w + gr) * 2560;
      pa[m] = __builtin_amdgcn_raw_buffer_load_b128(rs, vo, o, 0);
      pb[m] = __builtin_amdgcn_raw_buffer_load_b128(rs, vo, o + 1024, 0);
      pc[m] = __builtin_amdgcn_raw_buffer_load_b64(rs, lane * 8, o + 2048, 0);
    }
  };
  auto o_calc = [&](int m, int gr) {
    float ov[20];
#pragma unroll
    for (int hh = 0; hh < 4; ++hh) {
      v4u bf;
      if (hh == 0) bf = pa[m];
      else if (hh == 1) bf = v4u{pa[m].z, pa[m].w, pb[m].x, pb[m].y};
      else if (hh == 2) bf = pb[m];
      else bf = v4u{pb[m].z, pb[m].w, pc[m].x, pc[m].y};
      const f32x4 z4 = f32x4{0.f, 0.f, 0.f, 0.f};
      const f32x4 c0 = mfma32(kvf[hh][0], bf, z4);
      const f32x4 c1 = mfma32(kvf[hh][1], bf, z4);
      const float sc = gv[m].x * __builtin_amdgcn_rcpf(c1[1] + 1e-6f);
      ov[5 * hh + 0] = c0[0] * sc;
      ov[5 * hh + 1] = c0[1] * sc;
      ov[5 * hh + 2] = c0[2] * sc;
      ov[5 * hh + 3] = c0[3] * sc;
      ov[5 * hh + 4] = c1[0] * sc;
    }
    if (gr == 0) {
      of[m][0] = v4u{pack2(ov[0], ov[1]), pack2(ov[2], ov[3]), pack2(ov[4], ov[5]), pack2(ov[6], ov[7])};
      of[m][1] = v4u{pack2(ov[8], ov[9]), pack2(ov[10], ov[11]), pack2(ov[12], ov[13]), pack2(ov[14], ov[15])};
      of[m][2].x = pack2(ov[16], ov[17]);
      of[m][2].y = pack2(ov[18], ov[19]);
    } else {
      of[m][2].z = pack2(ov[0], ov[1]);
      of[m][2].w = pack2(ov[2], ov[3]);
      of[m][3] = v4u{pack2(ov[4], ov[5]), pack2(ov[6], ov[7]), pack2(ov[8], ov[9]), pack2(ov[10], ov[11])};
      of[m][4] = v4u{pack2(ov[12], ov[13]), pack2(ov[14], ov[15]), pack2(ov[16], ov[17]), pack2(ov[18], ov[19])};
    }
  };
  static_for<NSTEP + 5>([&](auto IC) {
    constexpr int i = decltype(IC)::value;
    if (i == NSTEP - 1 || i == NSTEP - 2) {
      const unsigned msk = cntw == 14 ? 0xFFFFFFFFu : 0u;
#pragma unroll
      for (int m = 0; m < 4; ++m) hf[i % DEPTH][m] &= v4u{msk, msk, msk, msk};
    }
    if (i == NSTEP) {
#pragma unroll
      for (int t = 0; t < 5; ++t)
#pragma unroll
        for (int m = 0; m < 4; ++m) acc[t][m] *= gv[m].y;
      __builtin_amdgcn_sched_barrier(0);
    }
#pragma unroll
    for (int t = 0; t < 5; ++t)
#pragma unroll
      for (int m = 0; m < 4; ++m) {
        acc[t][m] = mfma32(wf[i % DEPTH][t], i < NSTEP ? hf[i % DEPTH][m] : of[m][i < NSTEP ? 0 : i - NSTEP], acc[t][m]);
      }
    if (i + DEPTH < NSTEP + 5) ld_step(i + DEPTH, hf[i % DEPTH], wf[i % DEPTH]);
    if (i == 8) pollv = __hip_atomic_load(mycnt, __ATOMIC_RELAXED, __HIP_MEMORY_SCOPE_AGENT);
    if (i == 10) {
      bool ok = __all(pollv == 8u);
      for (int spins = 0; !ok && spins < 200000; ++spins) {
        __builtin_amdgcn_s_sleep(16);
        const unsigned c = __hip_atomic_load(mycnt, __ATOMIC_RELAXED, __HIP_MEMORY_SCOPE_AGENT);
        ok = __all(c == 8u);
      }
      __builtin_amdgcn_fence(__ATOMIC_ACQUIRE, "wavefront");
      if (!ok) {
        const int fb = (int)OFF_FB + b * 65536 + 8 * w * 2048;
        for (int it = 0; it < 8 * 8; ++it) {
          const int hl = it >> 3, ls = (it & 7) * 16 + (lane >> 2), jp = lane & 3;
          const unsigned v = kvf_pair(slab, 8 * w + hl, ls, jp);
          __builtin_amdgcn_raw_buffer_store_b32(v, rs, fb + hl * 2048 + ls * 16 + jp * 4, 0, AUX_SC1);
        }
        asm volatile("s_waitcnt vmcnt(0)" ::: "memory");
        kvbase = fb;
      }
      ld_half(0);
    }
    if (i == 12) {
#pragma unroll
      for (int m = 0; m < 4; ++m) { __builtin_amdgcn_sched_barrier(0); o_calc(m, 0); __builtin_amdgcn_sched_barrier(0); }
      ld_half(1);
    }
    if (i == 14) {
#pragma unroll
      for (int m = 0; m < 4; ++m) { __builtin_amdgcn_sched_barrier(0); o_calc(m, 1); __builtin_amdgcn_sched_barrier(0); }
    }
    __builtin_amdgcn_sched_barrier(0);
  });
  __builtin_amdgcn_sched_barrier(0);
  f32x4* red = (f32x4*)smem;
#pragma unroll
  for (int t = 0; t < 5; ++t)
#pragma unroll
    for (int m = 0; m < 4; ++m) red[w * 1600 + (m * 5 + t) * 64 + lane] = acc[t][m];
  v2u bb[5];
#pragma unroll
  for (int t = 0; t < 5; ++t) bb[t] = *(const v2u*)(p.b2 + 80 * nb + 16 * t + 4 * g);
  __syncthreads();
  const float omg = w == 0 ? gv[0].y : (w == 1 ? gv[1].y : (w == 2 ? gv[2].y : gv[3].y));
  u16* orow = p.out + (size_t)(64 * rb + 16 * w + li) * D_ + 80 * nb + 4 * g;
#pragma unroll
  for (int t = 0; t < 5; ++t) {
    f32x4 s = red[(w * 5 + t) * 64 + lane];
#pragma unroll
    for (int ww = 1; ww < 4; ++ww) s += red[ww * 1600 + (w * 5 + t) * 64 + lane];
    const float o0 = s[0] + omg * bf2f(bb[t].x & 0xFFFFu), o1 = s[1] + omg * bf2f(bb[t].x >> 16);
    const float o2 = s[2] + omg * bf2f(bb[t].y & 0xFFFFu), o3 = s[3] + omg * bf2f(bb[t].y >> 16);
    *(v2u*)(orow + 16 * t) = v2u{pack2(o0, o1), pack2(o2, o3)};
  }
  STAMP(6);
  STAMP_STORE(OFF_DBG2, 4);
}

extern "C" void kernel_launch(void* const* d_in, const int* in_sizes, int n_in, void* d_out, int out_size, void* d_ws,
                              size_t ws_size, hipStream_t stream) {
  Params p;
  p.x = (const u16*)d_in[0];
  p.wqkv = (const u16*)d_in[1];
  p.wo = (const u16*)d_in[2];
  p.w1 = (const u16*)d_in[4];
  p.b1 = (const u16*)d_in[5];
  p.w2 = (const u16*)d_in[6];
  p.b2 = (const u16*)d_in[7];
  p.gw = (const u16*)d_in[8];
  p.out = (u16*)d_out;
  p.ws = (unsigned char*)d_ws;
  (void)hipFuncSetAttribute((const void*)gm_k1_kernel, hipFuncAttributeMaxDynamicSharedMemorySize, K1_LDS);
  (void)hipFuncSetAttribute((const void*)gm_k2_kernel, hipFuncAttributeMaxDynamicSharedMemorySize, K2_LDS);
  Params1 p1;
  p1.p = p;
  constexpr TileMap tm = make_tilemap();
  p1.tm = tm;
  gm_k1_kernel<<<256, 768, K1_LDS, stream>>>(p1);
  gm_k2_kernel<<<256, 256, K2_LDS, stream>>>(p);
}
```

```cpp
#include <hip/hip_runtime.h>
#include <hip/hip_bf16.h>
#include <stdint.h>
#include <type_traits>
#include <utility>

namespace {

constexpr int S_ = 2048, D_ = 640, F_ = 1728;
typedef unsigned short u16;
typedef unsigned v4u __attribute__((ext_vector_type(4)));
typedef unsigned v2u __attribute__((ext_vector_type(2)));
typedef float f32x4 __attribute__((ext_vector_type(4)));
typedef float f32x2 __attribute__((ext_vector_type(2)));
typedef short bf16x8 __attribute__((ext_vector_type(8)));
typedef short bf16x4 __attribute__((ext_vector_type(4)));
typedef short v4i16_t __attribute__((ext_vector_type(4)));
typedef __bf16 bf2_t __attribute__((ext_vector_type(2)));
#define LAS __attribute__((address_space(3)))
#define GAS __attribute__((address_space(1)))
#define DI __device__ __forceinline__

constexpr size_t OFF_CNT = 0;
constexpr size_t OFF_G = 32 * 256;
constexpr size_t OFF_KVF = OFF_G + 2048 * 8;
constexpr size_t OFF_SLAB = OFF_KVF + 65536;
constexpr size_t SLAB_BYTES = ((size_t)11 * 32 * 420 * 4 + 255) / 256 * 256;
constexpr size_t OFF_W2F = OFF_SLAB + SLAB_BYTES;
constexpr size_t OFF_WOF = OFF_W2F + (size_t)8 * 54 * 5 * 1024;
constexpr size_t OFF_PQ = OFF_WOF + (size_t)8 * 20 * 5 * 1024;
constexpr size_t OFF_HF = OFF_PQ + (size_t)128 * 8 * 2560;
constexpr size_t OFF_FB = OFF_HF + (size_t)128 * 54 * 1024;
constexpr size_t OFF_DUMP = OFF_FB + (size_t)256 * 65536;
constexpr size_t WS_BYTES = OFF_DUMP + 65536;
static_assert(OFF_SLAB % 256 == 0 && OFF_W2F % 256 == 0 && OFF_PQ % 256 == 0 && OFF_HF % 256 == 0, "align");
static_assert(WS_BYTES < (1ull << 31), "int offsets");

struct Params {
  const u16* x;
  const u16* wqkv;
  const u16* wo;
  const u16* w1;
  const u16* b1;
  const u16* w2;
  const u16* b2;
  const u16* gw;
  u16* out;
  unsigned char* ws;
};

template <int... I, class Fn> DI void static_for_impl(std::integer_sequence<int, I...>, Fn&& fn) { (fn(std::integral_constant<int, I>{}), ...); }
template <int N, class Fn> DI void static_for(Fn&& fn) { static_for_impl(std::make_integer_sequence<int, N>{}, fn); }
#define ST(T, ptr, val) (*(T*)(ptr) = (val))
DI float bf2f(unsigned v) { return __builtin_bit_cast(float, v << 16); }
DI float rbf(float a) { return (float)(__bf16)a; }
DI unsigned pack2(float a, float b) {
  f32x2 v = {a, b};
  bf2_t r = __builtin_convertvector(v, bf2_t);
  return __builtin_bit_cast(unsigned, r);
}
DI float phi_f(float t) {
  const float e = __builtin_amdgcn_exp2f(t * 1.4426950408889634f);
  return (t > 0.f ? t + 1.f : e) + 1e-6f;
}
DI f32x2 gelu2(f32x2 x) {
  const f32x2 c1 = {-2.302208198f, -2.302208198f}, c2 = {-0.1029432397f, -0.1029432397f};
  const f32x2 e = x * (x * x * c2 + c1);
  const f32x2 d = f32x2{__builtin_amdgcn_exp2f(e.x), __builtin_amdgcn_exp2f(e.y)} + f32x2{1.f, 1.f};
  return x * f32x2{__builtin_amdgcn_rcpf(d.x), __builtin_amdgcn_rcpf(d.y)};
}
DI f32x4 mfma32(v4u a, v4u b, f32x4 c) {
  return __builtin_amdgcn_mfma_f32_16x16x32_bf16(__builtin_bit_cast(bf16x8, a), __builtin_bit_cast(bf16x8, b), c, 0, 0, 0);
}
DI f32x4 mfma16(v2u a, v2u b, f32x4 c) {
  return __builtin_amdgcn_mfma_f32_16x16x16bf16_1k(__builtin_bit_cast(bf16x4, a), __builtin_bit_cast(bf16x4, b), c, 0, 0, 0);
}
DI v2u tr_read(const unsigned char* p) {
  return __builtin_bit_cast(v2u, __builtin_amdgcn_ds_read_tr16_b64_v4i16((LAS v4i16_t*)p));
}
DI void glds16(unsigned voff, const void* sbase, unsigned lds_dst) {
  unsigned keep;
  asm volatile("s_mov_b32 %0, m0\n\ts_mov_b32 m0, %3\n\ts_nop 0\n\tglobal_load_lds_dwordx4 %1, %2\n\ts_mov_b32 m0, %0"
               : "=&s"(keep) : "v"(voff), "s"(sbase), "s"(lds_dst) : "memory");
}

struct TileMap { unsigned v[256]; };
constexpr TileMap make_tilemap() {
  TileMap m{};
  for (int i = 0; i < 256; ++i) m.v[i] = 0xFFFF;
  for (int half = 0; half < 2; ++half) {
    const int q0 = half ? 6 : 0, q1 = half ? 11 : 6, h0 = half ? 6 : 0, h1 = half ? 13 : 6;
    for (int xq = 0; xq < 4; ++xq) {
      int n = 0;
      const int x = half * 4 + xq;
      for (int c = 0; c < 2; ++c)
        for (int rt = q0; rt < q1; ++rt) m.v[x + 8 * n++] = (unsigned)((1 << 12) | (rt << 6) | (2 * xq + c));
      for (int rt = q0; rt < q1; ++rt) m.v[x + 8 * n++] = (unsigned)((0 << 12) | (rt << 6) | xq);
      for (int c = 0; c < 2; ++c)
        for (int rt = h0; rt < h1; ++rt) m.v[x + 8 * n++] = (unsigned)((2 << 12) | (rt << 6) | (2 * xq + c));
      for (int rt = h0; rt < h1; ++rt)
        if (((rt - h0) & 3) == xq) m.v[x + 8 * n++] = (unsigned)((2 << 12) | (rt << 6) | 8);
    }
  }
  int nidle = 0, nqh = 0;
  for (int b = 0; b < 256; ++b) {
    const unsigned t = m.v[b] & 0xFFFF;
    if (t == 0xFFFF) m.v[b] = t | (unsigned)(nidle++) << 16;
    else if ((t >> 12) != 1) m.v[b] = t | (unsigned)(nqh++) << 16;
  }
  return m;
}
struct Params1 {
  Params p;
  TileMap tm;
};

constexpr int K1_STAGE = 45056;
constexpr int K1_LDS = 3 * K1_STAGE + 512 + 16384;

DI void k1_side(const Params& p, unsigned char* smem, int w, const u16* bias, int unit);

template <int TYPE>
DI void k1_tile(const Params& p, unsigned char* smem, int rt, int ct, unsigned share) {
  constexpr int MT = TYPE == 2 ? 5 : 3, NTW = TYPE == 2 ? 3 : 5, WM = TYPE == 2 ? 2 : 4, WN = TYPE == 2 ? 4 : 2;
  constexpr int BM = MT * 16 * WM, BN = NTW * 16 * WN, NT = BN / 16, XB = BM * 128, NMM = MT * NTW;
  constexpr int NX = BM / 32, NW = NT / 2;
  constexpr int NRD = MT + 2 * NTW;
  static_assert(XB + 64 * BN * 2 == K1_STAGE && NX + NW == 11, "stage");
  const int tid = threadIdx.x, lane = tid & 63, w = __builtin_amdgcn_readfirstlane(tid >> 6);
  const int wm = w / WN, wn = w % WN, g = lane >> 4, li = lane & 15;
  if (w >= 10) {
    k1_side(p, smem, w - 10, TYPE == 2 ? p.b1 + 192 * ct : nullptr, TYPE == 1 ? -1 : (int)share * 2 + (w - 10));
    return;
  }
  const int rv = rt * BM, r0 = rv + BM <= S_ ? rv : S_ - BM;
  const u16* W = TYPE == 2 ? p.w1 : p.wqkv;
  constexpr int LDW = TYPE == 2 ? F_ : 3 * D_;
  const unsigned srcx = (unsigned)((r0 + (lane >> 3)) * D_ + 8 * ((lane & 7) ^ (lane >> 3))) * 2u;
  unsigned src[NW];
#pragma unroll
  for (int i = 0; i < NW; ++i) {
    const int cc = 64 * i + lane;
    const int kbl = cc / (16 * NT), c2 = cc % (16 * NT);
    const int nt = c2 >> 4, rowp = (c2 >> 1) & 7, half = c2 & 1;
    const int k = 8 * kbl + (rowp ^ (4 * kbl));
    const int col = 16 * nt + 8 * half;
    int gcol;
    if (TYPE == 0) gcol = 160 * ct + col;
    else if (TYPE == 1) {
      gcol = (col < 80 ? D_ + col : 2 * D_ + col - 80) + 80 * ct;
    } else gcol = 192 * ct + col;
    src[i] = (unsigned)(k * LDW + gcol) * 2u;
  }
  const unsigned lds0 = (unsigned)(uintptr_t)(LAS unsigned char*)smem;
  constexpr int WB = K1_STAGE - XB;
  auto issue1 = [&](int i, int wv, int kt, int st) {
    if (i < NX) glds16(srcx, p.x + kt * 64 + (wv * (BM / 4) + i * 8) * D_, lds0 + st * XB + (wv * BM * 2 + 64 * i) * 16);
    else glds16(src[i - NX], W + (size_t)(kt * 64 + wv * 16) * LDW, lds0 + 3 * XB + st * WB + (wv * 32 * NT + 64 * (i - NX)) * 16);
  };
  if (w >= 8) {
    const int q0 = 2 * (w - 8);
#pragma unroll
    for (int t = 1; t < 3; ++t)
#pragma unroll
      for (int i = 0; i < 22; ++i) issue1(i % 11, q0 + i / 11, t, t);
    __builtin_amdgcn_s_barrier();
    int bf = 0;
#pragma unroll 1
    for (int kt = 0; kt < 9; ++kt) {
      if (kt < 8) asm volatile("s_waitcnt vmcnt(22)" ::: "memory");
      else asm volatile("s_waitcnt vmcnt(0)" ::: "memory");
      __builtin_amdgcn_s_barrier();
      if (kt + 3 < 10) {
#pragma unroll
        for (int i = 0; i < 22; ++i) issue1(i % 11, q0 + i / 11, kt + 3, bf);
      }
      bf = bf == 2 ? 0 : bf + 1;
    }
    return;
  }
#pragma unroll
  for (int i = 0; i < 11; ++i)
    if ((i & 1) == (w & 1)) issue1(i, w >> 1, 0, 0);
  v4u pf[4];
  {
    const int gwv = blockIdx.x * 8 + w;
    const unsigned long long bq = (unsigned long long)p.wqkv, b1 = (unsigned long long)p.w1, bx = (unsigned long long)p.x;
    const int r = gwv < 976 ? gwv : 975;
    const unsigned long long a3 = r < 352 ? bq + (unsigned long long)(2048 + r) * 1024
                                : (r < 464 ? b1 + (unsigned long long)(2048 + r - 352) * 1024 : bx + (unsigned long long)(2048 + r - 464) * 1024);
    const unsigned long long ad[4] = {bq + (unsigned long long)gwv * 1024, b1 + (unsigned long long)gwv * 1024, bx + (unsigned long long)gwv * 1024, a3};
#pragma unroll
    for (int j = 0; j < 4; ++j) asm volatile("global_load_dwordx4 %0, %1, off" : "=v"(pf[j]) : "v"(ad[j] + lane * 16) : "memory");
  }
  const unsigned xoff0 = (unsigned)((wm * MT * 16 + li) * 128 + ((g ^ (lane & 7)) << 4));
  const unsigned woff0 = (unsigned)(3 * XB + g * NT * 256 + wn * NTW * 256 + (4 * (g & 1) + (li >> 2)) * 32 + (lane & 3) * 8);
  unsigned woffk[5];
#pragma unroll
  for (int b = 0; b < 5; ++b) {
    const int cc = 16 * b + 4 * (lane & 3);
    const int col = cc < 40 ? 40 * wn + cc : 40 + 40 * wn + cc;
    woffk[b] = (unsigned)(3 * XB + g * NT * 256 + (col >> 4) * 256 + (4 * (g & 1) + (li >> 2)) * 32 + (col & 15) * 2);
  }
  auto raw_tile = [&](int ww, int i) {
    return (f32x4*)(smem + (ww < 4 ? XB + ww * 10240 : 3 * XB + WB + (ww - 4) * 10240) + i * 1024);
  };
  unsigned goff[2] = {0u, 0u};
  if (TYPE == 1) {
#pragma unroll
    for (int q = 0; q < 4; ++q) {
      const int o = (4 * tid + q) % 840, hh = o / 420, rem = o % 420, dd = rem / 21, e = rem % 21;
      const int c1 = 20 * hh + dd, n1 = c1 >> 4, row = c1 & 15;
      const int c2 = 40 + 20 * hh + e, n2 = c2 >> 4;
      const int tile = e < 20 ? (n1 == 0 ? n2 - 2 : (n1 == 1 ? n2 : n2 + 2)) : 7 + n1;
      const int col = e < 20 ? (c2 & 15) : 0;
      goff[q >> 1] |= (unsigned)(tile * 256 + ((row >> 2) * 16 + col) * 4 + (row & 3)) << (16 * (q & 1));
    }
    asm volatile("" : "+v"(goff[0]), "+v"(goff[1]));
  }
  unsigned char* wsb = p.ws;
  asm volatile("" : "+s"(wsb));
  f32x4 acc[MT][NTW];
#pragma unroll
  for (int a = 0; a < MT; ++a)
#pragma unroll
    for (int b = 0; b < NTW; ++b) acc[a][b] = f32x4{0.f, 0.f, 0.f, 0.f};
  v4u xa[MT], xb[MT];
  v2u wa[NTW][2], wb[NTW][2];
  auto rd1 = [&](int r, int ks, int st, v4u (&xf)[MT], v2u (&wf)[NTW][2]) {
    if (r < MT) xf[r] = *(const v4u*)(smem + (xoff0 ^ (ks * 64)) + (st * XB + r * 2048));
    else {
      const int b = (r - MT) >> 1, hi = (r - MT) & 1;
      if (TYPE == 1) wf[b][hi] = tr_read(smem + (woffk[b < 5 ? b : 0] ^ (hi * 128)) + (st * WB + ks * 4 * NT * 256));
      else wf[b][hi] = tr_read(smem + (woff0 ^ (hi * 128)) + (st * WB + ks * 4 * NT * 256 + b * 256));
    }
  };
  auto mm = [&](int a, int b, v4u (&xf)[MT], v2u (&wf)[NTW][2]) {
    const v4u wv = v4u{wf[b][0].x, wf[b][0].y, wf[b][1].x, wf[b][1].y};
    acc[a][b] = TYPE == 1 ? mfma32(xf[a], wv, acc[a][b]) : mfma32(wv, xf[a], acc[a][b]);
  };
  asm volatile("s_waitcnt vmcnt(0)" : "+v"(pf[0]), "+v"(pf[1]), "+v"(pf[2]), "+v"(pf[3]) :: "memory");
  __builtin_amdgcn_s_barrier();
#pragma unroll
  for (int r = 0; r < NRD; ++r) rd1(r, 0, 0, xa, wa);
  auto body = [&](int kt, auto MODE, auto BUF) {
    constexpr int mode = decltype(MODE)::value, sb = decltype(BUF)::value, sn = (sb + 1) % 3;
    __builtin_amdgcn_sched_barrier(0);
#pragma unroll
    for (int i = 0; i < NMM; ++i) {
      mm(i / NTW, i % NTW, xa, wa);
      if (i < NRD) rd1(i, 1, sb, xb, wb);
      __builtin_amdgcn_sched_barrier(0);
    }
    if (mode != 3) {
      asm volatile("s_waitcnt lgkmcnt(0)" ::: "memory");
      __builtin_amdgcn_s_barrier();
    }
    __builtin_amdgcn_sched_barrier(0);
#pragma unroll
    for (int i = 0; i < NMM; ++i) {
      mm(i / NTW, i % NTW, xb, wb);
      if (mode != 3 && i < NRD) rd1(i, 0, sn, xa, wa);
      __builtin_amdgcn_sched_barrier(0);
    }
  };
#pragma unroll 1
  for (int kt = 0; kt < 9; kt += 3) {
    body(kt, std::integral_constant<int, 0>{}, std::integral_constant<int, 0>{});
    body(kt + 1, std::integral_constant<int, 0>{}, std::integral_constant<int, 1>{});
    body(kt + 2, std::integral_constant<int, 0>{}, std::integral_constant<int, 2>{});
  }
  body(9, std::integral_constant<int, 3>{}, std::integral_constant<int, 0>{});
  __builtin_amdgcn_sched_barrier(0);
  if (TYPE == 0) {
    const int gq = 2 * ct + wn;
#pragma unroll
    for (int a = 0; a < MT; ++a) {
      const int st = (r0 + wm * 48 + 16 * a) >> 4;
      {
        unsigned pk[5][2];
#pragma unroll
        for (int b = 0; b < 5; ++b) {
          pk[b][0] = pack2(phi_f(acc[a][b][0]), phi_f(acc[a][b][1]));
          pk[b][1] = pack2(phi_f(acc[a][b][2]), phi_f(acc[a][b][3]));
        }
        unsigned char* dst = wsb + (st * 16 >= rv ? (unsigned)OFF_PQ + (unsigned)(st * 8 + gq) * 2560u : (unsigned)OFF_DUMP);
        ST(v4u, dst + lane * 16, (v4u{pk[0][0], pk[0][1], pk[1][0], pk[1][1]}));
        ST(v4u, dst + 1024 + lane * 16, (v4u{pk[2][0], pk[2][1], pk[3][0], pk[3][1]}));
        ST(v2u, dst + 2048 + lane * 8, (v2u{pk[4][0], pk[4][1]}));
      }
    }
  } else if (TYPE == 2) {
    f32x2 bias[3][2];
#pragma unroll
    for (int b = 0; b < 3; ++b) {
      const v2u bb = *(const v2u*)(smem + 3 * K1_STAGE + (48 * wn + 16 * b + 4 * g) * 2);
      bias[b][0] = f32x2{bf2f(bb.x & 0xFFFFu), bf2f(bb.x >> 16)};
      bias[b][1] = f32x2{bf2f(bb.y & 0xFFFFu), bf2f(bb.y >> 16)};
    }
    const int ntg = 12 * ct + 3 * wn;
#pragma unroll
    for (int a = 0; a < MT; ++a) {
      const int st = (r0 + wm * 80 + 16 * a) >> 4;
      unsigned pk[3][2];
#pragma unroll
      for (int b = 0; b < 3; ++b)
#pragma unroll
        for (int hf = 0; hf < 2; ++hf) {
          const f32x2 y = gelu2(f32x2{acc[a][b][2 * hf], acc[a][b][2 * hf + 1]} + bias[b][hf]);
          pk[b][hf] = pack2(y.x, y.y);
        }
      unsigned char* dst = wsb + (st * 16 >= rv ? (unsigned)OFF_HF + (unsigned)(st * 54 + (ntg >> 1)) * 1024u : (unsigned)OFF_DUMP) + lane * 16;
      if ((wn & 1) == 0) {
        ST(v4u, dst, (v4u{pk[0][0], pk[0][1], pk[1][0], pk[1][1]}));
        ST(v2u, dst + 1024, (v2u{pk[2][0], pk[2][1]}));
      } else {
        ST(v2u, dst + 8, (v2u{pk[0][0], pk[0][1]}));
        ST(v4u, dst + 1024, (v4u{pk[1][0], pk[1][1], pk[2][0], pk[2][1]}));
      }
    }
  } else {
    const v2u ones = v2u{0x3F803F80u, 0x3F803F80u};
    v2u fr[3][5];
#pragma unroll
    for (int a = 0; a < 3; ++a) {
      const bool valid = (r0 + wm * 48 + 16 * a) >= rv;
#pragma unroll
      for (int b = 0; b < 5; ++b) {
        const bool isk = (16 * b + li) < 40;
        float v[4];
#pragma unroll
        for (int r = 0; r < 4; ++r) {
          float t = acc[a][b][r];
          if (b < 2) t = phi_f(t);
          else if (b == 2) t = isk ? phi_f(t) : t;
          v[r] = valid ? t : 0.f;
        }
        fr[a][b] = v2u{pack2(v[0], v[1]), pack2(v[2], v[3])};
      }
    }
    __builtin_amdgcn_sched_barrier(0);
    f32x4 d[10];
#pragma unroll
    for (int i = 0; i < 10; ++i) d[i] = f32x4{0.f, 0.f, 0.f, 0.f};
#pragma unroll
    for (int a = 0; a < 3; ++a) {
      d[0] = mfma16(fr[a][0], fr[a][2], d[0]);
      d[1] = mfma16(fr[a][0], fr[a][3], d[1]);
      d[2] = mfma16(fr[a][1], fr[a][2], d[2]);
      d[3] = mfma16(fr[a][1], fr[a][3], d[3]);
      d[4] = mfma16(fr[a][1], fr[a][4], d[4]);
      d[5] = mfma16(fr[a][2], fr[a][3], d[5]);
      d[6] = mfma16(fr[a][2], fr[a][4], d[6]);
      d[7] = mfma16(fr[a][0], ones, d[7]);
      d[8] = mfma16(fr[a][1], ones, d[8]);
      d[9] = mfma16(fr[a][2], ones, d[9]);
    }
    __builtin_amdgcn_sched_barrier(0);
#pragma unroll
    for (int i = 0; i < 10; ++i) raw_tile(w, i)[lane] = d[i];
    __syncthreads();
    if (tid < 420) {
      const int cwn = tid / 210;
      f32x4 sum;
#pragma unroll
      for (int q = 0; q < 4; ++q) {
        const unsigned off = (goff[q >> 1] >> (16 * (q & 1))) & 0xFFFFu;
        float a = 0.f;
#pragma unroll
        for (int m = 0; m < 4; ++m) a += ((const float*)raw_tile(2 * m + cwn, 0))[off];
        sum[q] = a;
      }
      ((f32x4*)(wsb + OFF_SLAB))[(size_t)(rt * 32 + 4 * ct) * 105 + tid] = sum;
    }
  }
}

DI void retile_load(const Params& p, int id, unsigned scr_l, int lane) {
  id = id < 740 ? id : 739;
  const int Tt = id / 10, c4 = id % 10;
#pragma unroll
  for (int i = 0; i < 4; ++i) {
    const int j = lane >> 3;
    int row;
    if (Tt < 54) row = 32 * Tt + 16 * (j >> 2) + 4 * i + (j & 3);
    else {
      const int U = Tt - 54, ww = U / 5, u = U % 5, vv = 8 * u + j;
      row = (8 * ww + vv / 5) * 20 + 5 * i + vv % 5;
    }
    glds16((unsigned)(row * D_ + 64 * c4 + 8 * (lane & 7)) * 2u, Tt < 54 ? p.w2 : p.wo, scr_l + i * 1024);
  }
}
DI void retile_store(const Params& p, int id, const unsigned char* scr, int lane) {
  const int Tt = id / 10, c4 = id % 10, g = lane >> 4, li = lane & 15;
#pragma unroll
  for (int t4 = 0; t4 < 4; ++t4) {
    const unsigned char* a = scr + (8 * g + (li >> 2)) * 128 + t4 * 32 + (lane & 3) * 8;
    const v2u lo = tr_read(a), hi = tr_read(a + 512);
    const int ntg = 4 * c4 + t4, nb = ntg / 5, t = ntg % 5;
    const size_t o = Tt < 54 ? OFF_W2F + (size_t)((nb * 54 + Tt) * 5 + t) * 1024 : OFF_WOF + (size_t)((nb * 20 + Tt - 54) * 5 + t) * 1024;
    ST(v4u, p.ws + (id < 740 ? o : OFF_DUMP) + lane * 16, (v4u{lo.x, lo.y, hi.x, hi.y}));
  }
}

DI void k1_side(const Params& p, unsigned char* smem, int w, const u16* bias, int unit) {
  const int tid = threadIdx.x, lane = tid & 63;
  const int gw = blockIdx.x * 2 + w, g = lane >> 4, li = lane & 15;
  unsigned char* scr = smem + 3 * K1_STAGE + 512 + w * 4096;
  if (unit >= 0) retile_load(p, unit, (unsigned)(uintptr_t)(LAS unsigned char*)scr, lane);
  v4u xv[5], gv[5];
#pragma unroll
  for (int i = 0; i < 5; ++i) {
    gv[i] = *(const v4u*)(p.gw + (i * 16 + li) * 8);
    xv[i] = *(const v4u*)(p.x + (size_t)(4 * gw + g) * D_ + (i * 16 + li) * 8);
  }
  unsigned bias2 = 0u;
  if (bias) bias2 = ((const unsigned*)bias)[w * 48 + (lane < 48 ? lane : 47)];
  asm volatile("s_waitcnt vmcnt(0)" ::: "memory");
  __builtin_amdgcn_sched_barrier(0);
  if (bias && lane < 48) ((unsigned*)(smem + 3 * K1_STAGE))[w * 48 + lane] = bias2;
  if (unit >= 0) retile_store(p, unit, scr, lane);
  float s = 0.f;
#pragma unroll
  for (int i = 0; i < 5; ++i)
#pragma unroll
    for (int q = 0; q < 4; ++q) {
      s = __builtin_fmaf(bf2f(xv[i][q] & 0xFFFFu), bf2f(gv[i][q] & 0xFFFFu), s);
      s = __builtin_fmaf(__builtin_bit_cast(float, xv[i][q] & 0xFFFF0000u), __builtin_bit_cast(float, gv[i][q] & 0xFFFF0000u), s);
    }
#pragma unroll
  for (int o = 8; o > 0; o >>= 1) s += __shfl_xor(s, o);
  if (li == 0) {
    const float lg = rbf(s);
    const float gg = rbf(1.f / (1.f + __expf(-lg)));
    *(f32x2*)(p.ws + OFF_G + (size_t)(4 * gw + g) * 8) = f32x2{gg, rbf(1.f - gg)};
  }
  if (blockIdx.x == 0 && w == 0 && lane < 32) *(unsigned*)(p.ws + OFF_CNT + lane * 256) = 0u;
  asm volatile("s_waitcnt lgkmcnt(0)" ::: "memory");
}

DI void k1_retile(const Params& p, unsigned char* smem, int rank, int w) {
  const int lane = threadIdx.x & 63, gw = rank * 10 + w;
  unsigned char* scr = smem + w * 12288;
  const unsigned scr_l = (unsigned)(uintptr_t)(LAS unsigned char*)scr;
#pragma unroll 1
  for (int round = 0; round < 2; ++round) {
#pragma unroll
    for (int it = 0; it < 3; ++it) retile_load(p, 322 + gw + 70 * (3 * round + it), scr_l + it * 4096, lane);
    asm volatile("s_waitcnt vmcnt(0)" ::: "memory");
    __builtin_amdgcn_sched_barrier(0);
#pragma unroll
    for (int it = 0; it < 3; ++it) retile_store(p, 322 + gw + 70 * (3 * round + it), scr + it * 4096, lane);
    asm volatile("s_waitcnt lgkmcnt(0)" ::: "memory");
    __builtin_amdgcn_sched_barrier(0);
  }
}

}
extern "C" __global__ void __launch_bounds__(768, 3) gm_k1_kernel(Params1 p1) {
  extern __shared__ __attribute__((aligned(16))) unsigned char smem[];
  const Params& p = p1.p;
  const unsigned t = p1.tm.v[blockIdx.x];
  if ((t & 0xFFFFu) != 0xFFFFu) {
    const int type = (t >> 12) & 15, rt = (t >> 6) & 63, ct = t & 63;
    if (type == 0) k1_tile<0>(p, smem, rt, ct, t >> 16);
    else if (type == 1) k1_tile<1>(p, smem, rt, ct, t >> 16);
    else k1_tile<2>(p, smem, rt, ct, t >> 16);
  } else {
    const int w = __builtin_amdgcn_readfirstlane(threadIdx.x >> 6);
    if (w >= 10) k1_side(p, smem, w - 10, nullptr, -1);
    else k1_retile(p, smem, (int)(t >> 16), w);
  }
}
namespace {

constexpr int AUX_SC1 = 16;
constexpr int RSRC_W3 = 0x00020000;
constexpr int K2_LDS = 4 * 25600;

DI unsigned kvf_pair(const float* slab, int head, int ls, int jp) {
  const int f = ls >> 6, l2 = ls & 63, rho = l2 & 15, g2 = l2 >> 4, hh = head & 3;
  int e;
  if (f == 0) e = 5 * (rho >> 2) + (rho & 3);
  else e = (rho & 3) == 0 ? 5 * (rho >> 2) + 4 : ((rho & 3) == 1 ? 20 : -1);
  float v[2];
#pragma unroll
  for (int q = 0; q < 2; ++q) {
    const int j = 2 * jp + q;
    const int d = 16 * (j >> 2) + 4 * g2 + (j & 3) - 4 * hh;
    const bool ok = (e >= 0) && (d >= 0) && (d < 20);
    const int off = ok ? d * 21 + e : 0;
    float s = 0.f;
#pragma unroll
    for (int pp = 0; pp < 11; ++pp) s += slab[(size_t)(pp * 32 + head) * 420 + off];
    v[q] = ok ? s : 0.f;
  }
  return pack2(v[0], v[1]);
}

}
extern "C" __global__ void __launch_bounds__(256, 1) gm_k2_kernel(Params p) {
  extern __shared__ __attribute__((aligned(16))) unsigned char smem[];
  const int tid = threadIdx.x, lane = tid & 63, w = __builtin_amdgcn_readfirstlane(tid >> 6);
  const int g = lane >> 4, li = lane & 15;
  const int b = blockIdx.x, bi = b >> 3;
  const int rb = 4 * (b & 7) + (bi >> 3), nb = bi & 7;
  const __amdgpu_buffer_rsrc_t rs = __builtin_amdgcn_make_buffer_rsrc((void*)p.ws, 0, (int)WS_BYTES, RSRC_W3);
  const float* slab = (const float*)(p.ws + OFF_SLAB);
  GAS unsigned* cnt = (GAS unsigned*)(p.ws + OFF_CNT);
  GAS unsigned* mycnt = cnt + (8 * w + (lane & 7)) * 64;
  constexpr int NSTEP = 14, DEPTH = 3;
  const int T0 = 14 * w;
  const int cntw = w < 3 ? 14 : 12;
  const int vo = lane * 16;
  const int hbase = (int)OFF_HF + (4 * rb * 54) * 1024;
  const int wbase = (int)OFF_W2F + (nb * 54 * 5) * 1024;
  v4u hf[DEPTH][4], wf[DEPTH][5];
  const int wobase = (int)OFF_WOF + ((nb * 20 + 5 * w) * 5) * 1024;
  auto ld_step = [&](int i, v4u (&h)[4], v4u (&ww)[5]) {
    if (i < NSTEP) {
      const int T = T0 + (i < 12 ? i : (i < cntw ? i : cntw - 1));
#pragma unroll
      for (int m = 0; m < 4; ++m) h[m] = __builtin_amdgcn_raw_buffer_load_b128(rs, vo, hbase + (m * 54 + T) * 1024, 0);
#pragma unroll
      for (int t = 0; t < 5; ++t) ww[t] = __builtin_amdgcn_raw_buffer_load_b128(rs, vo, wbase + (T * 5 + t) * 1024, 0);
    } else {
#pragma unroll
      for (int t = 0; t < 5; ++t) ww[t] = __builtin_amdgcn_raw_buffer_load_b128(rs, vo, wobase + ((i - NSTEP) * 5 + t) * 1024, 0);
    }
  };
  if (w == 3) {
    const int head = b >> 3, ls = (b & 7) * 16 + (lane >> 2), jp = lane & 3;
    const unsigned v = kvf_pair(slab, head, ls, jp);
    __builtin_amdgcn_raw_buffer_store_b32(v, rs, (int)OFF_KVF + head * 2048 + ls * 16 + jp * 4, 0, AUX_SC1);
    asm volatile("s_waitcnt vmcnt(0)" ::: "memory");
    if (lane == 0) __hip_atomic_fetch_add(cnt + head * 64, 1u, __ATOMIC_RELAXED, __HIP_MEMORY_SCOPE_AGENT);
  }
  __builtin_amdgcn_sched_barrier(0);
#pragma unroll
  for (int i = 0; i < DEPTH; ++i) ld_step(i, hf[i], wf[i]);
  f32x2 gv[4];
#pragma unroll
  for (int m = 0; m < 4; ++m) gv[m] = *(const f32x2*)(p.ws + OFF_G + (size_t)(64 * rb + 16 * m + li) * 8);
  __builtin_amdgcn_sched_barrier(0);
  f32x4 acc[5][4];
#pragma unroll
  for (int t = 0; t < 5; ++t)
#pragma unroll
    for (int m = 0; m < 4; ++m) acc[t][m] = f32x4{0.f, 0.f, 0.f, 0.f};
  v4u pa[4], pb[4], kvf[4][2], of[4][5];
  v2u pc[4];
  unsigned pollv = 0;
  int kvbase = (int)OFF_KVF + 8 * w * 2048;
  auto ld_half = [&](int gr) {
#pragma unroll
    for (int hh = 0; hh < 4; ++hh)
#pragma unroll
      for (int f = 0; f < 2; ++f)
        kvf[hh][f] = __builtin_amdgcn_raw_buffer_load_b128(rs, vo, kvbase + (4 * gr + hh) * 2048 + f * 1024, AUX_SC1);
#pragma unroll
    for (int m = 0; m < 4; ++m) {
      const int o = (int)OFF_PQ + ((4 * rb + m) * 8 + 2 * w + gr) * 2560;
      pa[m] = __builtin_amdgcn_raw_buffer_load_b128(rs, vo, o, 0);
      pb[m] = __builtin_amdgcn_raw_buffer_load_b128(rs, vo, o + 1024, 0);
      pc[m] = __builtin_amdgcn_raw_buffer_load_b64(rs, lane * 8, o + 2048, 0);
    }
  };
  auto o_calc = [&](int m, int gr) {
    float ov[20];
#pragma unroll
    for (int hh = 0; hh < 4; ++hh) {
      v4u bf;
      if (hh == 0) bf = pa[m];
      else if (hh == 1) bf = v4u{pa[m].z, pa[m].w, pb[m].x, pb[m].y};
      else if (hh == 2) bf = pb[m];
      else bf = v4u{pb[m].z, pb[m].w, pc[m].x, pc[m].y};
      const f32x4 z4 = f32x4{0.f, 0.f, 0.f, 0.f};
      const f32x4 c0 = mfma32(kvf[hh][0], bf, z4);
      const f32x4 c1 = mfma32(kvf[hh][1], bf, z4);
      const float sc = gv[m].x * __builtin_amdgcn_rcpf(c1[1] + 1e-6f);
      ov[5 * hh + 0] = c0[0] * sc;
      ov[5 * hh + 1] = c0[1] * sc;
      ov[5 * hh + 2] = c0[2] * sc;
      ov[5 * hh + 3] = c0[3] * sc;
      ov[5 * hh + 4] = c1[0] * sc;
    }
    if (gr == 0) {
      of[m][0] = v4u{pack2(ov[0], ov[1]), pack2(ov[2], ov[3]), pack2(ov[4], ov[5]), pack2(ov[6], ov[7])};
      of[m][1] = v4u{pack2(ov[8], ov[9]), pack2(ov[10], ov[11]), pack2(ov[12], ov[13]), pack2(ov[14], ov[15])};
      of[m][2].x = pack2(ov[16], ov[17]);
      of[m][2].y = pack2(ov[18], ov[19]);
    } else {
      of[m][2].z = pack2(ov[0], ov[1]);
      of[m][2].w = pack2(ov[2], ov[3]);
      of[m][3] = v4u{pack2(ov[4], ov[5]), pack2(ov[6], ov[7]), pack2(ov[8], ov[9]), pack2(ov[10], ov[11])};
      of[m][4] = v4u{pack2(ov[12], ov[13]), pack2(ov[14], ov[15]), pack2(ov[16], ov[17]), pack2(ov[18], ov[19])};
    }
  };
  static_for<NSTEP + 5>([&](auto IC) {
    constexpr int i = decltype(IC)::value;
    if (i == NSTEP - 1 || i == NSTEP - 2) {
      const unsigned msk = cntw == 14 ? 0xFFFFFFFFu : 0u;
#pragma unroll
      for (int m = 0; m < 4; ++m) hf[i % DEPTH][m] &= v4u{msk, msk, msk, msk};
    }
    if (i == NSTEP) {
#pragma unroll
      for (int t = 0; t < 5; ++t)
#pragma unroll
        for (int m = 0; m < 4; ++m) acc[t][m] *= gv[m].y;
      __builtin_amdgcn_sched_barrier(0);
    }
#pragma unroll
    for (int t = 0; t < 5; ++t)
#pragma unroll
      for (int m = 0; m < 4; ++m) {
        acc[t][m] = mfma32(wf[i % DEPTH][t], i < NSTEP ? hf[i % DEPTH][m] : of[m][i < NSTEP ? 0 : i - NSTEP], acc[t][m]);
      }
    if (i + DEPTH < NSTEP + 5) ld_step(i + DEPTH, hf[i % DEPTH], wf[i % DEPTH]);
    if (i == 8) pollv = __hip_atomic_load(mycnt, __ATOMIC_RELAXED, __HIP_MEMORY_SCOPE_AGENT);
    if (i == 10) {
      bool ok = __all(pollv == 8u);
      for (int spins = 0; !ok && spins < 200000; ++spins) {
        __builtin_amdgcn_s_sleep(16);
        const unsigned c = __hip_atomic_load(mycnt, __ATOMIC_RELAXED, __HIP_MEMORY_SCOPE_AGENT);
        ok = __all(c == 8u);
      }
      __builtin_amdgcn_fence(__ATOMIC_ACQUIRE, "wavefront");
      if (!ok) {
        const int fb = (int)OFF_FB + b * 65536 + 8 * w * 2048;
        for (int it = 0; it < 8 * 8; ++it) {
          const int hl = it >> 3, ls = (it & 7) * 16 + (lane >> 2), jp = lane & 3;
          const unsigned v = kvf_pair(slab, 8 * w + hl, ls, jp);
          __builtin_amdgcn_raw_buffer_store_b32(v, rs, fb + hl * 2048 + ls * 16 + jp * 4, 0, AUX_SC1);
        }
        asm volatile("s_waitcnt vmcnt(0)" ::: "memory");
        kvbase = fb;
      }
      ld_half(0);
    }
    if (i == 12) {
#pragma unroll
      for (int m = 0; m < 4; ++m) { __builtin_amdgcn_sched_barrier(0); o_calc(m, 0); __builtin_amdgcn_sched_barrier(0); }
      ld_half(1);
    }
    if (i == 14) {
#pragma unroll
      for (int m = 0; m < 4; ++m) { __builtin_amdgcn_sched_barrier(0); o_calc(m, 1); __builtin_amdgcn_sched_barrier(0); }
    }
    __builtin_amdgcn_sched_barrier(0);
  });
  __builtin_amdgcn_sched_barrier(0);
  f32x4* red = (f32x4*)smem;
#pragma unroll
  for (int t = 0; t < 5; ++t)
#pragma unroll
    for (int m = 0; m < 4; ++m) red[w * 1600 + (m * 5 + t) * 64 + lane] = acc[t][m];
  v2u bb[5];
#pragma unroll
  for (int t = 0; t < 5; ++t) bb[t] = *(const v2u*)(p.b2 + 80 * nb + 16 * t + 4 * g);
  __syncthreads();
  const float omg = w == 0 ? gv[0].y : (w == 1 ? gv[1].y : (w == 2 ? gv[2].y : gv[3].y));
  u16* orow = p.out + (size_t)(64 * rb + 16 * w + li) * D_ + 80 * nb + 4 * g;
#pragma unroll
  for (int t = 0; t < 5; ++t) {
    f32x4 s = red[(w * 5 + t) * 64 + lane];
#pragma unroll
    for (int ww = 1; ww < 4; ++ww) s += red[ww * 1600 + (w * 5 + t) * 64 + lane];
    const float o0 = s[0] + omg * bf2f(bb[t].x & 0xFFFFu), o1 = s[1] + omg * bf2f(bb[t].x >> 16);
    const float o2 = s[2] + omg * bf2f(bb[t].y & 0xFFFFu), o3 = s[3] + omg * bf2f(bb[t].y >> 16);
    *(v2u*)(orow + 16 * t) = v2u{pack2(o0, o1), pack2(o2, o3)};
  }
}

extern "C" void kernel_launch(void* const* d_in, const int* in_sizes, int n_in, void* d_out, int out_size, void* d_ws,
                              size_t ws_size, hipStream_t stream) {
  Params p;
  p.x = (const u16*)d_in[0];
  p.wqkv = (const u16*)d_in[1];
  p.wo = (const u16*)d_in[2];
  p.w1 = (const u16*)d_in[4];
  p.b1 = (const u16*)d_in[5];
  p.w2 = (const u16*)d_in[6];
  p.b2 = (const u16*)d_in[7];
  p.gw = (const u16*)d_in[8];
  p.out = (u16*)d_out;
  p.ws = (unsigned char*)d_ws;
  (void)hipFuncSetAttribute((const void*)gm_k1_kernel, hipFuncAttributeMaxDynamicSharedMemorySize, K1_LDS);
  (void)hipFuncSetAttribute((const void*)gm_k2_kernel, hipFuncAttributeMaxDynamicSharedMemorySize, K2_LDS);
  Params1 p1;
  p1.p = p;
  constexpr TileMap tm = make_tilemap();
  p1.tm = tm;
  gm_k1_kernel<<<256, 768, K1_LDS, stream>>>(p1);
  gm_k2_kernel<<<256, 256, K2_LDS, stream>>>(p);
}
```

```cpp
#include <hip/hip_runtime.h>
#include <hip/hip_bf16.h>
#include <stdint.h>
#include <type_traits>
#include <utility>

namespace {

constexpr int S_ = 2048, D_ = 640, F_ = 1728;
typedef unsigned short u16;
typedef unsigned v4u __attribute__((ext_vector_type(4)));
typedef unsigned v2u __attribute__((ext_vector_type(2)));
typedef float f32x4 __attribute__((ext_vector_type(4)));
typedef float f32x2 __attribute__((ext_vector_type(2)));
typedef short bf16x8 __attribute__((ext_vector_type(8)));
typedef short bf16x4 __attribute__((ext_vector_type(4)));
typedef short v4i16_t __attribute__((ext_vector_type(4)));
typedef __bf16 bf2_t __attribute__((ext_vector_type(2)));
#define LAS __attribute__((address_space(3)))
#define GAS __attribute__((address_space(1)))
#define DI __device__ __forceinline__

constexpr size_t OFF_CNT = 0;
constexpr size_t OFF_G = 32 * 256;
constexpr size_t OFF_KVF = OFF_G + 2048 * 8;
constexpr size_t OFF_SLAB = OFF_KVF + 65536;
constexpr size_t SLAB_BYTES = ((size_t)11 * 32 * 420 * 4 + 255) / 256 * 256;
constexpr size_t OFF_W2F = OFF_SLAB + SLAB_BYTES;
constexpr size_t OFF_WOF = OFF_W2F + (size_t)8 * 54 * 5 * 1024;
constexpr size_t OFF_PQ = OFF_WOF + (size_t)8 * 20 * 5 * 1024;
constexpr size_t OFF_HF = OFF_PQ + (size_t)128 * 8 * 2560;
constexpr size_t OFF_FB = OFF_HF + (size_t)128 * 54 * 1024;
constexpr size_t OFF_DUMP = OFF_FB + (size_t)256 * 65536;
constexpr size_t WS_BYTES = OFF_DUMP + 65536;
static_assert(OFF_SLAB % 256 == 0 && OFF_W2F % 256 == 0 && OFF_PQ % 256 == 0 && OFF_HF % 256 == 0, "align");
static_assert(WS_BYTES < (1ull << 31), "int offsets");

struct Params {
  const u16* x;
  const u16* wqkv;
  const u16* wo;
  const u16* w1;
  const u16* b1;
  const u16* w2;
  const u16* b2;
  const u16* gw;
  u16* out;
  unsigned char* ws;
};

template <int... I, class Fn> DI void static_for_impl(std::integer_sequence<int, I...>, Fn&& fn) { (fn(std::integral_constant<int, I>{}), ...); }
template <int N, class Fn> DI void static_for(Fn&& fn) { static_for_impl(std::make_integer_sequence<int, N>{}, fn); }
#define ST(T, ptr, val) (*(GAS T*)(ptr) = (val))
DI float bf2f(unsigned v) { return __builtin_bit_cast(float, v << 16); }
DI float rbf(float a) { return (float)(__bf16)a; }
DI unsigned pack2(float a, float b) {
  f32x2 v = {a, b};
  bf2_t r = __builtin_convertvector(v, bf2_t);
  return __builtin_bit_cast(unsigned, r);
}
DI float phi_f(float t) {
  const float e = __builtin_amdgcn_exp2f(t * 1.4426950408889634f);
  return (t > 0.f ? t + 1.f : e) + 1e-6f;
}
DI f32x2 phi2(f32x2 t) {
  const f32x2 l = t * f32x2{1.4426950408889634f, 1.4426950408889634f};
  const f32x2 e = f32x2{__builtin_amdgcn_exp2f(l.x), __builtin_amdgcn_exp2f(l.y)};
  const f32x2 u = t + f32x2{1.f, 1.f};
  return f32x2{t.x > 0.f ? u.x : e.x, t.y > 0.f ? u.y : e.y} + f32x2{1e-6f, 1e-6f};
}
DI f32x2 gelu2(f32x2 x) {
  const f32x2 c1 = {-2.302208198f, -2.302208198f}, c2 = {-0.1029432397f, -0.1029432397f};
  const f32x2 e = x * (x * x * c2 + c1);
  const f32x2 d = f32x2{__builtin_amdgcn_exp2f(e.x), __builtin_amdgcn_exp2f(e.y)} + f32x2{1.f, 1.f};
  return x * f32x2{__builtin_amdgcn_rcpf(d.x), __builtin_amdgcn_rcpf(d.y)};
}
DI f32x4 mfma32(v4u a, v4u b, f32x4 c) {
  return __builtin_amdgcn_mfma_f32_16x16x32_bf16(__builtin_bit_cast(bf16x8, a), __builtin_bit_cast(bf16x8, b), c, 0, 0, 0);
}
DI f32x4 mfma16(v2u a, v2u b, f32x4 c) {
  return __builtin_amdgcn_mfma_f32_16x16x16bf16_1k(__builtin_bit_cast(bf16x4, a), __builtin_bit_cast(bf16x4, b), c, 0, 0, 0);
}
DI v2u tr_read(const unsigned char* p) {
  return __builtin_bit_cast(v2u, __builtin_amdgcn_ds_read_tr16_b64_v4i16((LAS v4i16_t*)p));
}
DI void glds16(unsigned voff, const void* sbase, unsigned lds_dst) {
  unsigned keep;
  asm volatile("s_mov_b32 %0, m0\n\ts_mov_b32 m0, %3\n\ts_nop 0\n\tglobal_load_lds_dwordx4 %1, %2\n\ts_mov_b32 m0, %0"
               : "=&s"(keep) : "v"(voff), "s"(sbase), "s"(lds_dst) : "memory");
}

struct TileMap { unsigned v[256]; };
constexpr TileMap make_tilemap() {
  TileMap m{};
  for (int i = 0; i < 256; ++i) m.v[i] = 0xFFFF;
  for (int half = 0; half < 2; ++half) {
    const int q0 = half ? 6 : 0, q1 = half ? 11 : 6, h0 = half ? 6 : 0, h1 = half ? 13 : 6;
    for (int xq = 0; xq < 4; ++xq) {
      int n = 0;
      const int x = half * 4 + xq;
      for (int c = 0; c < 2; ++c)
        for (int rt = q0; rt < q1; ++rt) m.v[x + 8 * n++] = (unsigned)((1 << 12) | (rt << 6) | (2 * xq + c));
      for (int rt = q0; rt < q1; ++rt) m.v[x + 8 * n++] = (unsigned)((0 << 12) | (rt << 6) | xq);
      for (int c = 0; c < 2; ++c)
        for (int rt = h0; rt < h1; ++rt) m.v[x + 8 * n++] = (unsigned)((2 << 12) | (rt << 6) | (2 * xq + c));
      for (int rt = h0; rt < h1; ++rt)
        if (((rt - h0) & 3) == xq) m.v[x + 8 * n++] = (unsigned)((2 << 12) | (rt << 6) | 8);
    }
  }
  int nidle = 0, nqh = 0;
  for (int b = 0; b < 256; ++b) {
    const unsigned t = m.v[b] & 0xFFFF;
    if (t == 0xFFFF) m.v[b] = t | (unsigned)(nidle++) << 16;
    else if ((t >> 12) != 1) m.v[b] = t | (unsigned)(nqh++) << 16;
  }
  return m;
}
struct Params1 {
  Params p;
  TileMap tm;
};

constexpr int K1_STAGE = 45056;
constexpr int K1_LDS = 3 * K1_STAGE + 512 + 16384;

DI void k1_side(const Params& p, unsigned char* smem, int w, const u16* bias, int unit);

template <int TYPE>
DI void k1_tile(const Params& p, unsigned char* smem, int rt, int ct, unsigned share) {
  constexpr int MT = TYPE == 2 ? 5 : 3, NTW = TYPE == 2 ? 3 : 5, WM = TYPE == 2 ? 2 : 4, WN = TYPE == 2 ? 4 : 2;
  constexpr int BM = MT * 16 * WM, BN = NTW * 16 * WN, NT = BN / 16, XB = BM * 128, NMM = MT * NTW;
  constexpr int NX = BM / 32, NW = NT / 2;
  constexpr int NRD = MT + 2 * NTW;
  static_assert(XB + 64 * BN * 2 == K1_STAGE && NX + NW == 11, "stage");
  const int tid = threadIdx.x, lane = tid & 63, w = __builtin_amdgcn_readfirstlane(tid >> 6);
  const int wm = w / WN, wn = w % WN, g = lane >> 4, li = lane & 15;
  if (w >= 10) {
    k1_side(p, smem, w - 10, TYPE == 2 ? p.b1 + 192 * ct : nullptr, TYPE == 1 ? -1 : (int)share * 2 + (w - 10));
    return;
  }
  const int rv = rt * BM, r0 = rv + BM <= S_ ? rv : S_ - BM;
  const u16* W = TYPE == 2 ? p.w1 : p.wqkv;
  constexpr int LDW = TYPE == 2 ? F_ : 3 * D_;
  const unsigned srcx = (unsigned)((r0 + (lane >> 3)) * D_ + 8 * ((lane & 7) ^ (lane >> 3))) * 2u;
  unsigned src[NW];
#pragma unroll
  for (int i = 0; i < NW; ++i) {
    const int cc = 64 * i + lane;
    const int kbl = cc / (16 * NT), c2 = cc % (16 * NT);
    const int nt = c2 >> 4, rowp = (c2 >> 1) & 7, half = c2 & 1;
    const int k = 8 * kbl + (rowp ^ (4 * kbl));
    const int col = 16 * nt + 8 * half;
    int gcol;
    if (TYPE == 0) gcol = 160 * ct + col;
    else if (TYPE == 1) {
      gcol = (col < 80 ? D_ + col : 2 * D_ + col - 80) + 80 * ct;
    } else gcol = 192 * ct + col;
    src[i] = (unsigned)(k * LDW + gcol) * 2u;
  }
  const unsigned lds0 = (unsigned)(uintptr_t)(LAS unsigned char*)smem;
  constexpr int WB = K1_STAGE - XB;
  auto issue1 = [&](int i, int wv, int kt, int st) {
    if (i < NX) glds16(srcx, p.x + kt * 64 + (wv * (BM / 4) + i * 8) * D_, lds0 + st * XB + (wv * BM * 2 + 64 * i) * 16);
    else glds16(src[i - NX], W + (size_t)(kt * 64 + wv * 16) * LDW, lds0 + 3 * XB + st * WB + (wv * 32 * NT + 64 * (i - NX)) * 16);
  };
  if (w >= 8) {
    const int q0 = 2 * (w - 8);
#pragma unroll
    for (int t = 1; t < 3; ++t)
#pragma unroll
      for (int i = 0; i < 22; ++i) issue1(i % 11, q0 + i / 11, t, t);
    __builtin_amdgcn_s_barrier();
    int bf = 0;
#pragma unroll 1
    for (int kt = 0; kt < 9; ++kt) {
      if (kt < 8) asm volatile("s_waitcnt vmcnt(22)" ::: "memory");
      else asm volatile("s_waitcnt vmcnt(0)" ::: "memory");
      __builtin_amdgcn_s_barrier();
      if (kt + 3 < 10) {
#pragma unroll
        for (int i = 0; i < 22; ++i) issue1(i % 11, q0 + i / 11, kt + 3, bf);
      }
      bf = bf == 2 ? 0 : bf + 1;
    }
    return;
  }
#pragma unroll
  for (int i = 0; i < 11; ++i)
    if ((i & 1) == (w & 1)) issue1(i, w >> 1, 0, 0);
  v4u pf[4];
  {
    const int gwv = blockIdx.x * 8 + w;
    const unsigned long long bq = (unsigned long long)p.wqkv, b1 = (unsigned long long)p.w1, bx = (unsigned long long)p.x;
    const int r = gwv < 976 ? gwv : 975;
    const unsigned long long a3 = r < 352 ? bq + (unsigned long long)(2048 + r) * 1024
                                : (r < 464 ? b1 + (unsigned long long)(2048 + r - 352) * 1024 : bx + (unsigned long long)(2048 + r - 464) * 1024);
    const unsigned long long ad[4] = {bq + (unsigned long long)gwv * 1024, b1 + (unsigned long long)gwv * 1024, bx + (unsigned long long)gwv * 1024, a3};
#pragma unroll
    for (int j = 0; j < 4; ++j) asm volatile("global_load_dwordx4 %0, %1, off" : "=v"(pf[j]) : "v"(ad[j] + lane * 16) : "memory");
  }
  const unsigned xoff0 = (unsigned)((wm * MT * 16 + li) * 128 + ((g ^ (lane & 7)) << 4));
  const unsigned woff0 = (unsigned)(3 * XB + g * NT * 256 + wn * NTW * 256 + (4 * (g & 1) + (li >> 2)) * 32 + (lane & 3) * 8);
  unsigned woffk[5];
#pragma unroll
  for (int b = 0; b < 5; ++b) {
    const int cc = 16 * b + 4 * (lane & 3);
    const int col = cc < 40 ? 40 * wn + cc : 40 + 40 * wn + cc;
    woffk[b] = (unsigned)(3 * XB + g * NT * 256 + (col >> 4) * 256 + (4 * (g & 1) + (li >> 2)) * 32 + (col & 15) * 2);
  }
  auto raw_tile = [&](int ww, int i) {
    return (f32x4*)(smem + (ww < 4 ? XB + ww * 10240 : 3 * XB + WB + (ww - 4) * 10240) + i * 1024);
  };
  unsigned goff[2] = {0u, 0u};
  if (TYPE == 1) {
#pragma unroll
    for (int q = 0; q < 4; ++q) {
      const int o = (4 * tid + q) % 840, hh = o / 420, rem = o % 420, dd = rem / 21, e = rem % 21;
      const int c1 = 20 * hh + dd, n1 = c1 >> 4, row = c1 & 15;
      const int c2 = 40 + 20 * hh + e, n2 = c2 >> 4;
      const int tile = e < 20 ? (n1 == 0 ? n2 - 2 : (n1 == 1 ? n2 : n2 + 2)) : 7 + n1;
      const int col = e < 20 ? (c2 & 15) : 0;
      goff[q >> 1] |= (unsigned)(tile * 256 + ((row >> 2) * 16 + col) * 4 + (row & 3)) << (16 * (q & 1));
    }
    asm volatile("" : "+v"(goff[0]), "+v"(goff[1]));
  }
  GAS unsigned char* wsb = (GAS unsigned char*)p.ws;
  asm volatile("" : "+s"(wsb));
  f32x4 acc[MT][NTW];
#pragma unroll
  for (int a = 0; a < MT; ++a)
#pragma unroll
    for (int b = 0; b < NTW; ++b) acc[a][b] = f32x4{0.f, 0.f, 0.f, 0.f};
  v4u xa[MT], xb[MT];
  v2u wa[NTW][2], wb[NTW][2];
  auto rd1 = [&](int r, int ks, int st, v4u (&xf)[MT], v2u (&wf)[NTW][2]) {
    if (r < MT) xf[r] = *(const v4u*)(smem + (xoff0 ^ (ks * 64)) + (st * XB + r * 2048));
    else {
      const int b = (r - MT) >> 1, hi = (r - MT) & 1;
      if (TYPE == 1) wf[b][hi] = tr_read(smem + (woffk[b < 5 ? b : 0] ^ (hi * 128)) + (st * WB + ks * 4 * NT * 256));
      else wf[b][hi] = tr_read(smem + (woff0 ^ (hi * 128)) + (st * WB + ks * 4 * NT * 256 + b * 256));
    }
  };
  auto mm = [&](int a, int b, v4u (&xf)[MT], v2u (&wf)[NTW][2]) {
    const v4u wv = v4u{wf[b][0].x, wf[b][0].y, wf[b][1].x, wf[b][1].y};
    acc[a][b] = TYPE == 1 ? mfma32(xf[a], wv, acc[a][b]) : mfma32(wv, xf[a], acc[a][b]);
  };
  asm volatile("s_waitcnt vmcnt(0)" : "+v"(pf[0]), "+v"(pf[1]), "+v"(pf[2]), "+v"(pf[3]) :: "memory");
  __builtin_amdgcn_s_barrier();
#pragma unroll
  for (int r = 0; r < NRD; ++r) rd1(r, 0, 0, xa, wa);
  auto body = [&](int kt, auto MODE, auto BUF) {
    constexpr int mode = decltype(MODE)::value, sb = decltype(BUF)::value, sn = (sb + 1) % 3;
    __builtin_amdgcn_sched_barrier(0);
#pragma unroll
    for (int i = 0; i < NMM; ++i) {
      mm(i / NTW, i % NTW, xa, wa);
      if (i < NRD) rd1(i, 1, sb, xb, wb);
      __builtin_amdgcn_sched_barrier(0);
    }
    if (mode != 3) {
      asm volatile("s_waitcnt lgkmcnt(0)" ::: "memory");
      __builtin_amdgcn_s_barrier();
    }
    __builtin_amdgcn_sched_barrier(0);
#pragma unroll
    for (int i = 0; i < NMM; ++i) {
      mm(i / NTW, i % NTW, xb, wb);
      if (mode != 3 && i < NRD) rd1(i, 0, sn, xa, wa);
      __builtin_amdgcn_sched_barrier(0);
    }
  };
#pragma unroll 1
  for (int kt = 0; kt < 9; kt += 3) {
    body(kt, std::integral_constant<int, 0>{}, std::integral_constant<int, 0>{});
    body(kt + 1, std::integral_constant<int, 0>{}, std::integral_constant<int, 1>{});
    body(kt + 2, std::integral_constant<int, 0>{}, std::integral_constant<int, 2>{});
  }
  body(9, std::integral_constant<int, 3>{}, std::integral_constant<int, 0>{});
  __builtin_amdgcn_sched_barrier(0);
  if (TYPE == 0) {
    const int gq = 2 * ct + wn;
#pragma unroll
    for (int a = 0; a < MT; ++a) {
      const int st = (r0 + wm * 48 + 16 * a) >> 4;
      {
        unsigned pk[5][2];
#pragma unroll
        for (int b = 0; b < 5; ++b) {
          const f32x2 p0 = phi2(f32x2{acc[a][b][0], acc[a][b][1]}), p1 = phi2(f32x2{acc[a][b][2], acc[a][b][3]});
          pk[b][0] = pack2(p0.x, p0.y);
          pk[b][1] = pack2(p1.x, p1.y);
        }
        GAS unsigned char* dst = wsb + (st * 16 >= rv ? (unsigned)OFF_PQ + (unsigned)(st * 8 + gq) * 2560u : (unsigned)OFF_DUMP);
        ST(v4u, dst + lane * 16, (v4u{pk[0][0], pk[0][1], pk[1][0], pk[1][1]}));
        ST(v4u, dst + 1024 + lane * 16, (v4u{pk[2][0], pk[2][1], pk[3][0], pk[3][1]}));
        ST(v2u, dst + 2048 + lane * 8, (v2u{pk[4][0], pk[4][1]}));
      }
    }
  } else if (TYPE == 2) {
    f32x2 bias[3][2];
#pragma unroll
    for (int b = 0; b < 3; ++b) {
      const v2u bb = *(const v2u*)(smem + 3 * K1_STAGE + (48 * wn + 16 * b + 4 * g) * 2);
      bias[b][0] = f32x2{bf2f(bb.x & 0xFFFFu), bf2f(bb.x >> 16)};
      bias[b][1] = f32x2{bf2f(bb.y & 0xFFFFu), bf2f(bb.y >> 16)};
    }
    const int ntg = 12 * ct + 3 * wn;
#pragma unroll
    for (int a = 0; a < MT; ++a) {
      const int st = (r0 + wm * 80 + 16 * a) >> 4;
      unsigned pk[3][2];
#pragma unroll
      for (int b = 0; b < 3; ++b)
#pragma unroll
        for (int hf = 0; hf < 2; ++hf) {
          const f32x2 y = gelu2(f32x2{acc[a][b][2 * hf], acc[a][b][2 * hf + 1]} + bias[b][hf]);
          pk[b][hf] = pack2(y.x, y.y);
        }
      GAS unsigned char* dst = wsb + (st * 16 >= rv ? (unsigned)OFF_HF + (unsigned)(st * 54 + (ntg >> 1)) * 1024u : (unsigned)OFF_DUMP) + lane * 16;
      if ((wn & 1) == 0) {
        ST(v4u, dst, (v4u{pk[0][0], pk[0][1], pk[1][0], pk[1][1]}));
        ST(v2u, dst + 1024, (v2u{pk[2][0], pk[2][1]}));
      } else {
        ST(v2u, dst + 8, (v2u{pk[0][0], pk[0][1]}));
        ST(v4u, dst + 1024, (v4u{pk[1][0], pk[1][1], pk[2][0], pk[2][1]}));
      }
    }
  } else {
    const v2u ones = v2u{0x3F803F80u, 0x3F803F80u};
    v2u fr[3][5];
#pragma unroll
    for (int a = 0; a < 3; ++a) {
      const bool valid = (r0 + wm * 48 + 16 * a) >= rv;
#pragma unroll
      for (int b = 0; b < 5; ++b) {
        const bool isk = (16 * b + li) < 40;
        f32x2 v0 = f32x2{acc[a][b][0], acc[a][b][1]}, v1 = f32x2{acc[a][b][2], acc[a][b][3]};
        if (b <= 2) {
          const f32x2 p0 = phi2(v0), p1 = phi2(v1);
          if (b < 2 || isk) { v0 = p0; v1 = p1; }
        }
        const unsigned vm = valid ? 0xFFFFFFFFu : 0u;
        fr[a][b] = v2u{pack2(v0.x, v0.y) & vm, pack2(v1.x, v1.y) & vm};
      }
    }
    __builtin_amdgcn_sched_barrier(0);
    f32x4 d[10];
#pragma unroll
    for (int i = 0; i < 10; ++i) d[i] = f32x4{0.f, 0.f, 0.f, 0.f};
#pragma unroll
    for (int a = 0; a < 3; ++a) {
      d[0] = mfma16(fr[a][0], fr[a][2], d[0]);
      d[1] = mfma16(fr[a][0], fr[a][3], d[1]);
      d[2] = mfma16(fr[a][1], fr[a][2], d[2]);
      d[3] = mfma16(fr[a][1], fr[a][3], d[3]);
      d[4] = mfma16(fr[a][1], fr[a][4], d[4]);
      d[5] = mfma16(fr[a][2], fr[a][3], d[5]);
      d[6] = mfma16(fr[a][2], fr[a][4], d[6]);
      d[7] = mfma16(fr[a][0], ones, d[7]);
      d[8] = mfma16(fr[a][1], ones, d[8]);
      d[9] = mfma16(fr[a][2], ones, d[9]);
    }
    __builtin_amdgcn_sched_barrier(0);
#pragma unroll
    for (int i = 0; i < 10; ++i) raw_tile(w, i)[lane] = d[i];
    __syncthreads();
    if (tid < 420) {
      const int cwn = tid / 210;
      f32x4 sum;
#pragma unroll
      for (int q = 0; q < 4; ++q) {
        const unsigned off = (goff[q >> 1] >> (16 * (q & 1))) & 0xFFFFu;
        float a = 0.f;
#pragma unroll
        for (int m = 0; m < 4; ++m) a += ((const float*)raw_tile(2 * m + cwn, 0))[off];
        sum[q] = a;
      }
      ((GAS f32x4*)(wsb + OFF_SLAB))[(size_t)(rt * 32 + 4 * ct) * 105 + tid] = sum;
    }
  }
}

DI void retile_load(const Params& p, int id, unsigned scr_l, int lane) {
  id = id < 740 ? id : 739;
  const int Tt = id / 10, c4 = id % 10;
#pragma unroll
  for (int i = 0; i < 4; ++i) {
    const int j = lane >> 3;
    int row;
    if (Tt < 54) row = 32 * Tt + 16 * (j >> 2) + 4 * i + (j & 3);
    else {
      const int U = Tt - 54, ww = U / 5, u = U % 5, vv = 8 * u + j;
      row = (8 * ww + vv / 5) * 20 + 5 * i + vv % 5;
    }
    glds16((unsigned)(row * D_ + 64 * c4 + 8 * (lane & 7)) * 2u, Tt < 54 ? p.w2 : p.wo, scr_l + i * 1024);
  }
}
DI void retile_store(const Params& p, int id, const unsigned char* scr, int lane) {
  const int Tt = id / 10, c4 = id % 10, g = lane >> 4, li = lane & 15;
#pragma unroll
  for (int t4 = 0; t4 < 4; ++t4) {
    const unsigned char* a = scr + (8 * g + (li >> 2)) * 128 + t4 * 32 + (lane & 3) * 8;
    const v2u lo = tr_read(a), hi = tr_read(a + 512);
    const int ntg = 4 * c4 + t4, nb = ntg / 5, t = ntg % 5;
    const size_t o = Tt < 54 ? OFF_W2F + (size_t)((nb * 54 + Tt) * 5 + t) * 1024 : OFF_WOF + (size_t)((nb * 20 + Tt - 54) * 5 + t) * 1024;
    ST(v4u, p.ws + (id < 740 ? o : OFF_DUMP) + lane * 16, (v4u{lo.x, lo.y, hi.x, hi.y}));
  }
}

DI void k1_side(const Params& p, unsigned char* smem, int w, const u16* bias, int unit) {
  const int tid = threadIdx.x, lane = tid & 63;
  const int gw = blockIdx.x * 2 + w, g = lane >> 4, li = lane & 15;
  unsigned char* scr = smem + 3 * K1_STAGE + 512 + w * 4096;
  if (unit >= 0) retile_load(p, unit, (unsigned)(uintptr_t)(LAS unsigned char*)scr, lane);
  v4u xv[5], gv[5];
#pragma unroll
  for (int i = 0; i < 5; ++i) {
    gv[i] = *(const v4u*)(p.gw + (i * 16 + li) * 8);
    xv[i] = *(const v4u*)(p.x + (size_t)(4 * gw + g) * D_ + (i * 16 + li) * 8);
  }
  unsigned bias2 = 0u;
  if (bias) bias2 = ((const unsigned*)bias)[w * 48 + (lane < 48 ? lane : 47)];
  asm volatile("s_waitcnt vmcnt(0)" ::: "memory");
  __builtin_amdgcn_sched_barrier(0);
  if (bias && lane < 48) ((unsigned*)(smem + 3 * K1_STAGE))[w * 48 + lane] = bias2;
  if (unit >= 0) retile_store(p, unit, scr, lane);
  float s = 0.f;
#pragma unroll
  for (int i = 0; i < 5; ++i)
#pragma unroll
    for (int q = 0; q < 4; ++q) {
      s = __builtin_fmaf(bf2f(xv[i][q] & 0xFFFFu), bf2f(gv[i][q] & 0xFFFFu), s);
      s = __builtin_fmaf(__builtin_bit_cast(float, xv[i][q] & 0xFFFF0000u), __builtin_bit_cast(float, gv[i][q] & 0xFFFF0000u), s);
    }
#pragma unroll
  for (int o = 8; o > 0; o >>= 1) s += __shfl_xor(s, o);
  if (li == 0) {
    const float lg = rbf(s);
    const float gg = rbf(1.f / (1.f + __expf(-lg)));
    *(f32x2*)(p.ws + OFF_G + (size_t)(4 * gw + g) * 8) = f32x2{gg, rbf(1.f - gg)};
  }
  if (blockIdx.x == 0 && w == 0 && lane < 32) *(unsigned*)(p.ws + OFF_CNT + lane * 256) = 0u;
  asm volatile("s_waitcnt lgkmcnt(0)" ::: "memory");
}

DI void k1_retile(const Params& p, unsigned char* smem, int rank, int w) {
  const int lane = threadIdx.x & 63, gw = rank * 10 + w;
  unsigned char* scr = smem + w * 12288;
  const unsigned scr_l = (unsigned)(uintptr_t)(LAS unsigned char*)scr;
#pragma unroll 1
  for (int round = 0; round < 2; ++round) {
#pragma unroll
    for (int it = 0; it < 3; ++it) retile_load(p, 322 + gw + 70 * (3 * round + it), scr_l + it * 4096, lane);
    asm volatile("s_waitcnt vmcnt(0)" ::: "memory");
    __builtin_amdgcn_sched_barrier(0);
#pragma unroll
    for (int it = 0; it < 3; ++it) retile_store(p, 322 + gw + 70 * (3 * round + it), scr + it * 4096, lane);
    asm volatile("s_waitcnt lgkmcnt(0)" ::: "memory");
    __builtin_amdgcn_sched_barrier(0);
  }
}

}
extern "C" __global__ void __launch_bounds__(768, 3) gm_k1_kernel(Params1 p1) {
  extern __shared__ __attribute__((aligned(16))) unsigned char smem[];
  const Params& p = p1.p;
  const unsigned t = p1.tm.v[blockIdx.x];
  if ((t & 0xFFFFu) != 0xFFFFu) {
    const int type = (t >> 12) & 15, rt = (t >> 6) & 63, ct = t & 63;
    if (type == 0) k1_tile<0>(p, smem, rt, ct, t >> 16);
    else if (type == 1) k1_tile<1>(p, smem, rt, ct, t >> 16);
    else k1_tile<2>(p, smem, rt, ct, t >> 16);
  } else {
    const int w = __builtin_amdgcn_readfirstlane(threadIdx.x >> 6);
    if (w >= 10) k1_side(p, smem, w - 10, nullptr, -1);
    else k1_retile(p, smem, (int)(t >> 16), w);
  }
}
namespace {

constexpr int AUX_SC1 = 16;
constexpr int RSRC_W3 = 0x00020000;
constexpr int K2_LDS = 4 * 25600;

DI unsigned kvf_pair(const float* slab, int head, int ls, int jp) {
  const int f = ls >> 6, l2 = ls & 63, rho = l2 & 15, g2 = l2 >> 4, hh = head & 3;
  int e;
  if (f == 0) e = 5 * (rho >> 2) + (rho & 3);
  else e = (rho & 3) == 0 ? 5 * (rho >> 2) + 4 : ((rho & 3) == 1 ? 20 : -1);
  float v[2];
#pragma unroll
  for (int q = 0; q < 2; ++q) {
    const int j = 2 * jp + q;
    const int d = 16 * (j >> 2) + 4 * g2 + (j & 3) - 4 * hh;
    const bool ok = (e >= 0) && (d >= 0) && (d < 20);
    const int off = ok ? d * 21 + e : 0;
    float s = 0.f;
#pragma unroll
    for (int pp = 0; pp < 11; ++pp) s += slab[(size_t)(pp * 32 + head) * 420 + off];
    v[q] = ok ? s : 0.f;
  }
  return pack2(v[0], v[1]);
}

}
extern "C" __global__ void __launch_bounds__(256, 1) gm_k2_kernel(Params p) {
  extern __shared__ __attribute__((aligned(16))) unsigned char smem[];
  const int tid = threadIdx.x, lane = tid & 63, w = __builtin_amdgcn_readfirstlane(tid >> 6);
  const int g = lane >> 4, li = lane & 15;
  const int b = blockIdx.x, bi = b >> 3;
  const int rb = 4 * (b & 7) + (bi >> 3), nb = bi & 7;
  const __amdgpu_buffer_rsrc_t rs = __builtin_amdgcn_make_buffer_rsrc((void*)p.ws, 0, (int)WS_BYTES, RSRC_W3);
  const float* slab = (const float*)(p.ws + OFF_SLAB);
  GAS unsigned* cnt = (GAS unsigned*)(p.ws + OFF_CNT);
  GAS unsigned* mycnt = cnt + (8 * w + (lane & 7)) * 64;
  constexpr int NSTEP = 14, DEPTH = 3;
  const int T0 = 14 * w;
  const int cntw = w < 3 ? 14 : 12;
  const int vo = lane * 16;
  const int hbase = (int)OFF_HF + (4 * rb * 54) * 1024;
  const int wbase = (int)OFF_W2F + (nb * 54 * 5) * 1024;
  v4u hf[DEPTH][4], wf[DEPTH][5];
  const int wobase = (int)OFF_WOF + ((nb * 20 + 5 * w) * 5) * 1024;
  auto ld_step = [&](int i, v4u (&h)[4], v4u (&ww)[5]) {
    if (i < NSTEP) {
      const int T = T0 + (i < 12 ? i : (i < cntw ? i : cntw - 1));
#pragma unroll
      for (int m = 0; m < 4; ++m) h[m] = __builtin_amdgcn_raw_buffer_load_b128(rs, vo, hbase + (m * 54 + T) * 1024, 0);
#pragma unroll
      for (int t = 0; t < 5; ++t) ww[t] = __builtin_amdgcn_raw_buffer_load_b128(rs, vo, wbase + (T * 5 + t) * 1024, 0);
    } else {
#pragma unroll
      for (int t = 0; t < 5; ++t) ww[t] = __builtin_amdgcn_raw_buffer_load_b128(rs, vo, wobase + ((i - NSTEP) * 5 + t) * 1024, 0);
    }
  };
  if (w == 3) {
    const int head = b >> 3, ls = (b & 7) * 16 + (lane >> 2), jp = lane & 3;
    const unsigned v = kvf_pair(slab, head, ls, jp);
    __builtin_amdgcn_raw_buffer_store_b32(v, rs, (int)OFF_KVF + head * 2048 + ls * 16 + jp * 4, 0, AUX_SC1);
    asm volatile("s_waitcnt vmcnt(0)" ::: "memory");
    if (lane == 0) __hip_atomic_fetch_add(cnt + head * 64, 1u, __ATOMIC_RELAXED, __HIP_MEMORY_SCOPE_AGENT);
  }
  __builtin_amdgcn_sched_barrier(0);
#pragma unroll
  for (int i = 0; i < DEPTH; ++i) ld_step(i, hf[i], wf[i]);
  f32x2 gv[4];
#pragma unroll
  for (int m = 0; m < 4; ++m) gv[m] = *(const f32x2*)(p.ws + OFF_G + (size_t)(64 * rb + 16 * m + li) * 8);
  __builtin_amdgcn_sched_barrier(0);
  f32x4 acc[5][4];
#pragma unroll
  for (int t = 0; t < 5; ++t)
#pragma unroll
    for (int m = 0; m < 4; ++m) acc[t][m] = f32x4{0.f, 0.f, 0.f, 0.f};
  v4u pa[4], pb[4], kvf[4][2], of[4][5];
  v2u pc[4];
  unsigned pollv = 0;
  int kvbase = (int)OFF_KVF + 8 * w * 2048;
  auto ld_half = [&](int gr) {
#pragma unroll
    for (int hh = 0; hh < 4; ++hh)
#pragma unroll
      for (int f = 0; f < 2; ++f)
        kvf[hh][f] = __builtin_amdgcn_raw_buffer_load_b128(rs, vo, kvbase + (4 * gr + hh) * 2048 + f * 1024, AUX_SC1);
#pragma unroll
    for (int m = 0; m < 4; ++m) {
      const int o = (int)OFF_PQ + ((4 * rb + m) * 8 + 2 * w + gr) * 2560;
      pa[m] = __builtin_amdgcn_raw_buffer_load_b128(rs, vo, o, 0);
      pb[m] = __builtin_amdgcn_raw_buffer_load_b128(rs, vo, o + 1024, 0);
      pc[m] = __builtin_amdgcn_raw_buffer_load_b64(rs, lane * 8, o + 2048, 0);
    }
  };
  auto o_calc = [&](int m, int gr) {
    float ov[20];
#pragma unroll
    for (int hh = 0; hh < 4; ++hh) {
      v4u bf;
      if (hh == 0) bf = pa[m];
      else if (hh == 1) bf = v4u{pa[m].z, pa[m].w, pb[m].x, pb[m].y};
      else if (hh == 2) bf = pb[m];
      else bf = v4u{pb[m].z, pb[m].w, pc[m].x, pc[m].y};
      const f32x4 z4 = f32x4{0.f, 0.f, 0.f, 0.f};
      const f32x4 c0 = mfma32(kvf[hh][0], bf, z4);
      const f32x4 c1 = mfma32(kvf[hh][1], bf, z4);
      const float sc = gv[m].x * __builtin_amdgcn_rcpf(c1[1] + 1e-6f);
      ov[5 * hh + 0] = c0[0] * sc;
      ov[5 * hh + 1] = c0[1] * sc;
      ov[5 * hh + 2] = c0[2] * sc;
      ov[5 * hh + 3] = c0[3] * sc;
      ov[5 * hh + 4] = c1[0] * sc;
    }
    if (gr == 0) {
      of[m][0] = v4u{pack2(ov[0], ov[1]), pack2(ov[2], ov[3]), pack2(ov[4], ov[5]), pack2(ov[6], ov[7])};
      of[m][1] = v4u{pack2(ov[8], ov[9]), pack2(ov[10], ov[11]), pack2(ov[12], ov[13]), pack2(ov[14], ov[15])};
      of[m][2].x = pack2(ov[16], ov[17]);
      of[m][2].y = pack2(ov[18], ov[19]);
    } else {
      of[m][2].z = pack2(ov[0], ov[1]);
      of[m][2].w = pack2(ov[2], ov[3]);
      of[m][3] = v4u{pack2(ov[4], ov[5]), pack2(ov[6], ov[7]), pack2(ov[8], ov[9]), pack2(ov[10], ov[11])};
      of[m][4] = v4u{pack2(ov[12], ov[13]), pack2(ov[14], ov[15]), pack2(ov[16], ov[17]), pack2(ov[18], ov[19])};
    }
  };
  static_for<NSTEP + 5>([&](auto IC) {
    constexpr int i = decltype(IC)::value;
    if (i == NSTEP - 1 || i == NSTEP - 2) {
      const unsigned msk = cntw == 14 ? 0xFFFFFFFFu : 0u;
#pragma unroll
      for (int m = 0; m < 4; ++m) hf[i % DEPTH][m] &= v4u{msk, msk, msk, msk};
    }
    if (i == NSTEP) {
#pragma unroll
      for (int t = 0; t < 5; ++t)
#pragma unroll
        for (int m = 0; m < 4; ++m) acc[t][m] *= gv[m].y;
      __builtin_amdgcn_sched_barrier(0);
    }
#pragma unroll
    for (int t = 0; t < 5; ++t)
#pragma unroll
      for (int m = 0; m < 4; ++m) {
        acc[t][m] = mfma32(wf[i % DEPTH][t], i < NSTEP ? hf[i % DEPTH][m] : of[m][i < NSTEP ? 0 : i - NSTEP], acc[t][m]);
      }
    if (i + DEPTH < NSTEP + 5) ld_step(i + DEPTH, hf[i % DEPTH], wf[i % DEPTH]);
    if (i == 8) pollv = __hip_atomic_load(mycnt, __ATOMIC_RELAXED, __HIP_MEMORY_SCOPE_AGENT);
    if (i == 10) {
      bool ok = __all(pollv == 8u);
      for (int spins = 0; !ok && spins < 200000; ++spins) {
        __builtin_amdgcn_s_sleep(16);
        const unsigned c = __hip_atomic_load(mycnt, __ATOMIC_RELAXED, __HIP_MEMORY_SCOPE_AGENT);
        ok = __all(c == 8u);
      }
      __builtin_amdgcn_fence(__ATOMIC_ACQUIRE, "wavefront");
      if (!ok) {
        const int fb = (int)OFF_FB + b * 65536 + 8 * w * 2048;
        for (int it = 0; it < 8 * 8; ++it) {
          const int hl = it >> 3, ls = (it & 7) * 16 + (lane >> 2), jp = lane & 3;
          const unsigned v = kvf_pair(slab, 8 * w + hl, ls, jp);
          __builtin_amdgcn_raw_buffer_store_b32(v, rs, fb + hl * 2048 + ls * 16 + jp * 4, 0, AUX_SC1);
        }
        asm volatile("s_waitcnt vmcnt(0)" ::: "memory");
        kvbase = fb;
      }
      ld_half(0);
    }
    if (i == 12) {
#pragma unroll
      for (int m = 0; m < 4; ++m) { __builtin_amdgcn_sched_barrier(0); o_calc(m, 0); __builtin_amdgcn_sched_barrier(0); }
      ld_half(1);
    }
    if (i == 14) {
#pragma unroll
      for (int m = 0; m < 4; ++m) { __builtin_amdgcn_sched_barrier(0); o_calc(m, 1); __builtin_amdgcn_sched_barrier(0); }
    }
    __builtin_amdgcn_sched_barrier(0);
  });
  __builtin_amdgcn_sched_barrier(0);
  f32x4* red = (f32x4*)smem;
#pragma unroll
  for (int t = 0; t < 5; ++t)
#pragma unroll
    for (int m = 0; m < 4; ++m) red[w * 1600 + (m * 5 + t) * 64 + lane] = acc[t][m];
  v2u bb[5];
#pragma unroll
  for (int t = 0; t < 5; ++t) bb[t] = *(const v2u*)(p.b2 + 80 * nb + 16 * t + 4 * g);
  __syncthreads();
  const float omg = w == 0 ? gv[0].y : (w == 1 ? gv[1].y : (w == 2 ? gv[2].y : gv[3].y));
  u16* orow = p.out + (size_t)(64 * rb + 16 * w + li) * D_ + 80 * nb + 4 * g;
#pragma unroll
  for (int t = 0; t < 5; ++t) {
    f32x4 s = red[(w * 5 + t) * 64 + lane];
#pragma unroll
    for (int ww = 1; ww < 4; ++ww) s += red[ww * 1600 + (w * 5 + t) * 64 + lane];
    const float o0 = s[0] + omg * bf2f(bb[t].x & 0xFFFFu), o1 = s[1] + omg * bf2f(bb[t].x >> 16);
    const float o2 = s[2] + omg * bf2f(bb[t].y & 0xFFFFu), o3 = s[3] + omg * bf2f(bb[t].y >> 16);
    *(v2u*)(orow + 16 * t) = v2u{pack2(o0, o1), pack2(o2, o3)};
  }
}

extern "C" void kernel_launch(void* const* d_in, const int* in_sizes, int n_in, void* d_out, int out_size, void* d_ws,
                              size_t ws_size, hipStream_t stream) {
  Params p;
  p.x = (const u16*)d_in[0];
  p.wqkv = (const u16*)d_in[1];
  p.wo = (const u16*)d_in[2];
  p.w1 = (const u16*)d_in[4];
  p.b1 = (const u16*)d_in[5];
  p.w2 = (const u16*)d_in[6];
  p.b2 = (const u16*)d_in[7];
  p.gw = (const u16*)d_in[8];
  p.out = (u16*)d_out;
  p.ws = (unsigned char*)d_ws;
  (void)hipFuncSetAttribute((const void*)gm_k1_kernel, hipFuncAttributeMaxDynamicSharedMemorySize, K1_LDS);
  (void)hipFuncSetAttribute((const void*)gm_k2_kernel, hipFuncAttributeMaxDynamicSharedMemorySize, K2_LDS);
  Params1 p1;
  p1.p = p;
  constexpr TileMap tm = make_tilemap();
  p1.tm = tm;
  gm_k1_kernel<<<256, 768, K1_LDS, stream>>>(p1);
  gm_k2_kernel<<<256, 256, K2_LDS, stream>>>(p);
}
```
